```python
import torch

def _softmax(x, axis=-1):
    return torch.softmax(x, dim=axis)
def _gelu(x):
    return torch.nn.functional.gelu(x, approximate="tanh")
def _silu(x):
    return torch.nn.functional.silu(x)
def _sigmoid(x):
    return torch.sigmoid(x)
def _layernorm(x, axis=-1, eps=1e-5):
    m = x.mean(dim=axis, keepdim=True)
    v = x.var(dim=axis, keepdim=True, unbiased=False)
    return (x - m) / torch.sqrt(v + eps)
def _scatter_add(tgt, idx, src, axis=0):
    return tgt.clone().index_add_(axis, idx, src)
def _segment_sum(src, seg, n):
    out = torch.zeros((n,) + src.shape[1:], dtype=src.dtype)
    return out.index_add_(0, seg, src)
def _pad(x, pw):
    flat = tuple(v for pair in reversed(pw) for v in pair)
    return torch.nn.functional.pad(x, flat)

kernel_name = 'gather_scatter__cheap_equivariant_egnn__graph'

def setup_inputs():

    _w = (lambda a: (lambda t: t.to(torch.bfloat16) if t.is_floating_point() else t)(torch.as_tensor(a)))
    N, deg, D, De = 10000, 16, 256, max(16, 1)
    E = N * deg
    return {
        "h": _w((lambda *s: torch.randn(*s, dtype=torch.bfloat16) * 0.1)(N, D)),
        "pos": _w((lambda *s: torch.randn(*s, dtype=torch.bfloat16) * 0.8)(N, 3)),
        "e_attr": _w((lambda *s: torch.randn(*s, dtype=torch.bfloat16) * 0.8)(E, De)),
        "src": _w((lambda lo, hi, *s: torch.randint(lo, hi, s))(0, N, E).to(torch.int64)),
        "dst": _w((lambda lo, hi, *s: torch.randint(lo, hi, s))(0, N, E).to(torch.int64)),
        "w_e": _w(0.1 * (lambda *s: torch.randn(*s, dtype=torch.bfloat16) * 0.8)(2 * D + 1 + De, D)),
        "w_x": _w(0.1 * (lambda *s: torch.randn(*s, dtype=torch.bfloat16) * 0.1)(D, 1)),
        "w_h": _w(0.1 * (lambda *s: torch.randn(*s, dtype=torch.bfloat16) * 0.1)(2 * D, D)),
    }

def reference(h, pos, e_attr, src, dst, w_e, w_x, w_h):
    N, D = h.shape[0], h.shape[1]
    diff = pos[dst] - pos[src]
    dist2 = torch.sum(diff * diff, axis=-1, keepdims=True)
    e_in = torch.concatenate([h[dst], h[src], dist2, e_attr], axis=-1)
    m_ij = _silu(e_in @ w_e)
    coord_scale = m_ij @ w_x
    pos_new = pos + _segment_sum(diff * coord_scale, dst, N)
    m_i = _segment_sum(m_ij, dst, N)
    h_new = h + _silu(torch.concatenate([h, m_i], axis=-1) @ w_h)
    return h_new, pos_new
```

```cpp
#include <hip/hip_runtime.h>
#include <stdint.h>

typedef short bf16x8 __attribute__((ext_vector_type(8)));
typedef float f32x16 __attribute__((ext_vector_type(16)));
typedef float f32x4 __attribute__((ext_vector_type(4)));
typedef float f32x2 __attribute__((ext_vector_type(2)));
typedef unsigned u32x4 __attribute__((ext_vector_type(4)));
typedef unsigned u32x2 __attribute__((ext_vector_type(2)));
typedef int i32x4 __attribute__((ext_vector_type(4)));
typedef __bf16 bf16v2 __attribute__((ext_vector_type(2)));
#define DI __device__ __forceinline__
#define LDSP(T, off) ((T __attribute__((address_space(3)))*)(uintptr_t)(off))

constexpr int NN = 10000, NE = 160000;
constexpr int NBLK = 256;
constexpr int NCH = 64;
constexpr int CHE = NE / NCH;
constexpr int MAXT = 25;
constexpr float NLOG2E = -1.4426950408889634f;
constexpr float NLN2 = -0.6931471805599453f;

constexpr size_t WS_B = 0;
constexpr size_t WS_REC = WS_B + (size_t)NN * 512;
constexpr size_t WS_TBL = WS_REC + (size_t)NE * 8;
constexpr size_t WS_W1 = WS_TBL + (size_t)NBLK * NCH * 16;
constexpr size_t WS_WH = WS_W1 + 131072;
constexpr size_t WS_SM = WS_WH + 262144;

constexpr unsigned PAIR_PITCH = 1040;
constexpr unsigned SLOT_B = 16 * PAIR_PITCH;
constexpr unsigned SLOT_SZ = SLOT_B + 1024;
constexpr unsigned NSLOT = 5;
constexpr unsigned L_RING = 0;
constexpr unsigned L_SD = L_RING + NSLOT * SLOT_SZ;
constexpr unsigned L_POOL = L_SD + 8 * 3072;
constexpr unsigned L_MI = L_POOL + 4 * 3072;
constexpr unsigned L_REC = L_MI + 40 * 512;
constexpr unsigned L_PP = L_REC + MAXT * 32 * 16;
constexpr unsigned L_POS = L_PP + 8 * 40 * 16;
constexpr unsigned L_END = L_POS + 256;
static_assert(L_END <= 163840, "LDS");

DI float bf2f(unsigned b) { return __uint_as_float(b << 16); }
DI unsigned pk2(float a, float b) {
  f32x2 v; v.x = a; v.y = b;
  return __builtin_bit_cast(unsigned, __builtin_convertvector(v, bf16v2));
}
DI float rbf(float a) { return __uint_as_float(pk2(a, 0.f) << 16); }
DI bf16x8 asb(u32x4 v) { return __builtin_bit_cast(bf16x8, v); }
DI f32x16 mfma32(u32x4 a, u32x4 b, f32x16 c) { return __builtin_amdgcn_mfma_f32_32x32x16_bf16(asb(a), asb(b), c, 0, 0, 0); }
DI f32x4 mfma16(u32x4 a, u32x4 b, f32x4 c) { return __builtin_amdgcn_mfma_f32_16x16x32_bf16(asb(a), asb(b), c, 0, 0, 0); }
DI int blk_start(int b) { return 39 * b + (b < 16 ? b : 16); }

DI void dma16(const void* base, unsigned voff, unsigned lds) {
  unsigned keep;
  asm volatile("s_mov_b32 %0, m0\n\ts_mov_b32 m0, %3\n\ts_nop 0\n\tglobal_load_lds_dwordx4 %1, %2\n\ts_mov_b32 m0, %0"
               : "=&s"(keep) : "v"(voff), "s"(base), "s"(lds) : "memory");
}
#define WAIT_VM(n) asm volatile("s_waitcnt vmcnt(" #n ")" ::: "memory")
#define WAIT_LGKM0() asm volatile("s_waitcnt lgkmcnt(0)" ::: "memory")
#define BAR() do { asm volatile("s_waitcnt lgkmcnt(0)" ::: "memory"); __builtin_amdgcn_s_barrier(); asm volatile("" ::: "memory"); } while (0)

#define ABL 0
#define STAMP(k)
#define RSTAMP(k)
DI unsigned scan64(unsigned v) {
  v += __builtin_amdgcn_update_dpp(0u, v, 0x111, 0xf, 0xf, false);
  v += __builtin_amdgcn_update_dpp(0u, v, 0x112, 0xf, 0xf, false);
  v += __builtin_amdgcn_update_dpp(0u, v, 0x114, 0xf, 0xf, false);
  v += __builtin_amdgcn_update_dpp(0u, v, 0x118, 0xf, 0xf, false);
  v += __builtin_amdgcn_update_dpp(0u, v, 0x142, 0xa, 0xf, false);
  v += __builtin_amdgcn_update_dpp(0u, v, 0x143, 0xc, 0xf, false);
  return v;
}

DI void tr8x2(const unsigned (&w)[8], u32x4& f0, u32x4& f1) {
#pragma unroll
  for (int j = 0; j < 4; j++) {
    f0[j] = __builtin_amdgcn_perm(w[2 * j + 1], w[2 * j], 0x05040100u);
    f1[j] = __builtin_amdgcn_perm(w[2 * j + 1], w[2 * j], 0x07060302u);
  }
}

__global__ __launch_bounds__(256) void egnn_prep(const unsigned short* __restrict__ h, const int* __restrict__ src,
                                                 const int* __restrict__ dst, const unsigned short* __restrict__ we,
                                                 const unsigned short* __restrict__ wx, const unsigned short* __restrict__ wh,
                                                 char* __restrict__ ws) {
  __shared__ __attribute__((aligned(16))) char lds[40960];
  const int tid = threadIdx.x, lane = tid & 63;
  const int wv = __builtin_amdgcn_readfirstlane(tid >> 6);
#define STAMPBASE 256
  RSTAMP(30); STAMP(0);
  if (blockIdx.x >= NCH) {
    const int g = blockIdx.x - NCH;
    const int rt = ((g >> 4) << 3) + (g & 7), half = (g >> 3) & 1;
    if (rt >= NN / 80) return;
    const unsigned lbase = (unsigned)(uintptr_t)(__attribute__((address_space(3))) char*)lds;
    {
      const int hh = lane >> 5, l32 = lane & 31;
#pragma unroll
      for (int i = 0; i < 10; i++) {
        const int j = wv + 4 * i, row = 2 * j + hh;
        dma16(h, (unsigned)((rt * 80 + row) * 512 + ((l32 ^ (row & 15)) << 4)), lbase + j * 1024);
      }
    }
    const int c = lane & 15, q = lane >> 4;
    const unsigned* wp = (const unsigned*)(we + (size_t)(256 + 8 * q) * 256 + half * 128 + wv * 32 + 2 * c);
    u32x4 wf[8][2];
#pragma unroll
    for (int s = 0; s < 8; s++) {
      unsigned w[8];
#pragma unroll
      for (int j = 0; j < 8; j++) w[j] = wp[(32 * s + j) * 128];
      tr8x2(w, wf[s][0], wf[s][1]);
    }
    WAIT_VM(0);
    STAMP(1);
    BAR();
    STAMP(2);
    char* bp = ws + WS_B + (size_t)(rt * 80 + c) * 512 + half * 256 + wv * 64 + q * 16;
#pragma unroll
    for (int t = 0; t < 5; t++) {
      f32x4 a0 = {0.f, 0.f, 0.f, 0.f}, a1 = {0.f, 0.f, 0.f, 0.f};
#pragma unroll
      for (int s = 0; s < 8; s++) {
        const u32x4 hf = *(const u32x4*)(lds + (16 * t + c) * 512 + (((4 * s + q) ^ c) << 4));
        a0 = mfma16(wf[s][0], hf, a0);
        a1 = mfma16(wf[s][1], hf, a1);
      }
      u32x4 o;
#pragma unroll
      for (int i = 0; i < 4; i++) o[i] = pk2(a0[i] * NLOG2E, a1[i] * NLOG2E);
      *(u32x4*)(bp + t * 16 * 512) = o;
    }
    STAMP(3);
    WAIT_VM(0);
    STAMP(4); RSTAMP(31);
    return;
  }
  const int ch = blockIdx.x;
  unsigned* cnt = (unsigned*)lds;
  unsigned* wsum = (unsigned*)(lds + 5120);
  u32x2* stage = (u32x2*)(lds + 6144);
  i32x4 d4[3], s4[3];
#pragma unroll
  for (int r = 0; r < 3; r++) {
    const int i = tid + 256 * r;
    if (i < CHE / 4) {
      d4[r] = ((const i32x4*)(dst + ch * CHE))[i];
      s4[r] = ((const i32x4*)(src + ch * CHE))[i];
    }
  }
  {
    const int task = ch * 256 + tid;
    if (task < 12288) {
      const bool is1 = task < 4096;
      const int tt = is1 ? task : task - 4096;
      const int kg = tt >> 7, cp = tt & 127;
      const unsigned* wp = (const unsigned*)((is1 ? we : wh) + (size_t)(8 * kg) * 256 + 2 * cp);
      unsigned w[8];
#pragma unroll
      for (int j = 0; j < 8; j++) w[j] = wp[j * 128];
      u32x4 f[2];
      tr8x2(w, f[0], f[1]);
      const int s = kg >> 2, q = kg & 3;
#pragma unroll
      for (int u = 0; u < 2; u++) {
        const int col = 2 * cp + u, sl = col >> 5, f5 = col & 31;
        if (is1) {
          const int tau = f5 >> 4, cc = f5 & 15;
          *(u32x4*)(ws + WS_W1 + (size_t)((((sl * 2 + tau) * 8 + s) * 64) + q * 16 + cc) * 16) = f[u];
        } else {
          const int tau = (f5 >> 2) & 1, r = ((f5 >> 3) << 2) + (f5 & 3);
          *(u32x4*)(ws + WS_WH + (size_t)((((sl * 2 + tau) * 16 + s) * 64) + q * 16 + r) * 16) = f[u];
        }
      }
    } else if (task < 12288 + 512) {
      const int tt = task - 12288, sl = tt >> 6, ln = tt & 63, hh = ln >> 5, col = sl * 32 + (ln & 31);
      u32x4 hi, lo, wd = {0u, 0u, 0u, 0u}, x0, x1;
#pragma unroll
      for (int j = 0; j < 4; j++) {
        const float a = bf2f(we[(size_t)(513 + 8 * hh + 2 * j) * 256 + col]) * NLOG2E;
        const float b = bf2f(we[(size_t)(514 + 8 * hh + 2 * j) * 256 + col]) * NLOG2E;
        hi[j] = pk2(a, b);
        lo[j] = pk2(a - rbf(a), b - rbf(b));
        const int f0 = sl * 32 + 8 * (j >> 1) + 4 * hh + 2 * (j & 1);
        x0[j] = (unsigned)wx[f0] | ((unsigned)wx[f0 + 1] << 16);
        x1[j] = (unsigned)wx[f0 + 16] | ((unsigned)wx[f0 + 17] << 16);
      }
      if (hh == 0) {
        const float a = bf2f(we[(size_t)512 * 256 + col]) * NLOG2E;
        wd[0] = pk2(a, a - rbf(a));
      }
      u32x4* o = (u32x4*)(ws + WS_SM) + sl * 320 + ln;
      o[0] = hi; o[64] = lo; o[128] = wd; o[192] = x0; o[256] = x1;
    }
  }
  for (int i = tid; i < 1280; i += 256) cnt[i] = 0;
  STAMP(1);
  __syncthreads();
  unsigned key[12], rank[12];
#pragma unroll
  for (int r = 0; r < 3; r++) {
    if (tid + 256 * r < CHE / 4) {
#pragma unroll
      for (int u = 0; u < 4; u++) {
        const unsigned n = (unsigned)d4[r][u];
        unsigned b, loc;
        if (n < 640u) { b = n / 40u; loc = n - b * 40u; } else { b = (n - 640u) / 39u; loc = (n - 640u) - b * 39u; b += 16u; }
        const unsigned bin = b * 5u + (loc >> 3);
        key[4 * r + u] = bin | (loc << 16);
        rank[4 * r + u] = atomicAdd(&cnt[bin], 1u);
      }
    }
  }
  STAMP(2);
  __syncthreads();
  {
    unsigned c[5], tot = 0;
#pragma unroll
    for (int w = 0; w < 5; w++) { c[w] = cnt[5 * tid + w]; tot += c[w]; }
    const unsigned inc = scan64(tot);
    if (lane == 63) wsum[wv] = inc;
    __syncthreads();
    unsigned o = inc - tot;
#pragma unroll
    for (int w = 0; w < 3; w++) if (w < wv) o += wsum[w];
    unsigned of[6];
#pragma unroll
    for (int w = 0; w < 5; w++) { of[w] = o; cnt[5 * tid + w] = o; o += c[w]; }
    of[5] = o;
    u32x4 ent = {of[0] | (of[1] << 16), of[2] | (of[3] << 16), of[4] | (of[5] << 16), 0u};
    *(u32x4*)(ws + WS_TBL + (size_t)(tid * NCH + ch) * 16) = ent;
  }
  __syncthreads();
#pragma unroll
  for (int r = 0; r < 3; r++) {
    if (tid + 256 * r < CHE / 4) {
#pragma unroll
      for (int u = 0; u < 4; u++) {
        const unsigned k = key[4 * r + u], bin = k & 0xffffu, loc = k >> 16;
        const unsigned e = (unsigned)(ch * CHE + (tid + 256 * r) * 4 + u);
        u32x2 rec;
        rec.x = (e << 5) | (loc & 7u) | ((loc >> 3) << 23);
        rec.y = (unsigned)s4[r][u] << 9;
        stage[cnt[bin] + rank[4 * r + u]] = rec;
      }
    }
  }
  __syncthreads();
  STAMP(3);
  for (int i = tid; i < CHE / 2; i += 256) ((u32x4*)(ws + WS_REC + (size_t)ch * CHE * 8))[i] = ((const u32x4*)stage)[i];
  WAIT_VM(0);
  STAMP(4); RSTAMP(31);
}

DI void geom(unsigned long long pd, unsigned long long ps, unsigned& z, unsigned& w) {
  const float dx = rbf(bf2f((unsigned)pd & 0xffffu) - bf2f((unsigned)ps & 0xffffu));
  const float dy = rbf(bf2f((unsigned)(pd >> 16) & 0xffffu) - bf2f((unsigned)(ps >> 16) & 0xffffu));
  const float dz = rbf(bf2f((unsigned)(pd >> 32) & 0xffffu) - bf2f((unsigned)(ps >> 32) & 0xffffu));
  const float d2 = rbf(dx * dx) + rbf(dy * dy) + rbf(dz * dz);
  z = pk2(d2, dx);
  w = pk2(dy, dz);
}
DI unsigned long long load_pos(const char* pos, unsigned n) {
  const unsigned a = 6u * n;
  const u32x2 v = *(const u32x2*)(pos + (a & ~3u));
  const unsigned long long x = (unsigned long long)v.x | ((unsigned long long)v.y << 32);
  return x >> ((a & 2u) << 3);
}
DI unsigned long long lds_pos(unsigned loc) {
  const unsigned a = L_POS + 6u * loc;
  const unsigned long long x = (unsigned long long)*LDSP(unsigned, a & ~3u) | ((unsigned long long)*LDSP(unsigned, (a & ~3u) + 4u) << 32);
  return x >> ((a & 2u) << 3);
}

DI void prep_tile(int t, unsigned win, bool with_d2, unsigned dst, int lane) {
  const int rho = lane & 31, hh = lane >> 5;
  const u32x4 r = *LDSP(u32x4, L_REC + (unsigned)(t * 32 + rho) * 16u);
  const u32x4 z4 = {0u, 0u, 0u, 0u};
  *LDSP(u32x4, dst + 1024u + lane * 16u) = z4;
  *LDSP(u32x4, dst + 2048u + lane * 16u) = z4;
  const bool mine = (r.x >> 23) == win;
  const unsigned slot = r.x & 7u;
  const unsigned t4 = slot - 4u * hh;
  const unsigned long long oh = (mine && t4 < 4u) ? (0x3F80ull << (16u * t4)) : 0ull;
  u32x4 d;
  d.x = (unsigned)oh; d.y = (unsigned)(oh >> 32);
  d.z = (hh == 0 && with_d2) ? __builtin_amdgcn_perm(r.z, r.z, 0x01000100u) : 0u;
  d.w = 0u;
  *LDSP(u32x4, dst + lane * 16u) = d;
  if (mine) {
    const unsigned val = hh ? r.w : ((r.z & 0xffff0000u) | 0x3F80u);
    const unsigned r15 = rho & 15;
    const unsigned a = dst + 1024u + (unsigned)(rho >> 4) * 1024u + (((r15 >> 2) & 1u) * 32u + slot * 4u + 2u * hh) * 16u +
                       (((r15 >> 3) << 2) + (r15 & 3u)) * 2u;
    *LDSP(unsigned short, a) = (unsigned short)val;
    *LDSP(unsigned short, a + 16u) = (unsigned short)(val >> 16);
  }
}

DI void flush_window(const f32x16& acc, unsigned win, int wv, int lane, const u32x4& wx0, const u32x4& wx1) {
  u32x4 p0, p1;
#pragma unroll
  for (int d = 0; d < 4; d++) {
    p0[d] = pk2(acc[2 * d] * NLN2, acc[2 * d + 1] * NLN2);
    p1[d] = pk2(acc[8 + 2 * d] * NLN2, acc[9 + 2 * d] * NLN2);
  }
  f32x16 z = {0.f, 0.f, 0.f, 0.f, 0.f, 0.f, 0.f, 0.f, 0.f, 0.f, 0.f, 0.f, 0.f, 0.f, 0.f, 0.f};
  z = mfma32(wx0, p0, z);
  z = mfma32(wx1, p1, z);
  const unsigned row = win * 8u + ((lane & 31) >> 2);
  if ((lane & 3) == 0) {
    const unsigned base = L_MI + row * 512u + (lane >> 5) * 8u;
    const unsigned sw = row & 15u, c0 = wv * 4u;
    u32x2 v;
    v.x = p0[0]; v.y = p0[1]; *LDSP(u32x2, base + (((c0 + 0u) ^ sw) << 4)) = v;
    v.x = p0[2]; v.y = p0[3]; *LDSP(u32x2, base + (((c0 + 1u) ^ sw) << 4)) = v;
    v.x = p1[0]; v.y = p1[1]; *LDSP(u32x2, base + (((c0 + 2u) ^ sw) << 4)) = v;
    v.x = p1[2]; v.y = p1[3]; *LDSP(u32x2, base + (((c0 + 3u) ^ sw) << 4)) = v;
  }
  if (lane < 32) *LDSP(float, L_PP + wv * 640u + win * 128u + lane * 4u) = z[0];
}

__global__ __launch_bounds__(512, 2) void egnn_edge(const unsigned short* __restrict__ h, const char* __restrict__ pos,
                                                    const char* __restrict__ eattr, const unsigned short* __restrict__ we,
                                                    const unsigned short* __restrict__ wx, const char* __restrict__ ws,
                                                    unsigned short* __restrict__ out) {
  extern __shared__ __attribute__((aligned(16))) char smem[];
  const int tid = threadIdx.x, lane = tid & 63;
  const int wv = __builtin_amdgcn_readfirstlane(tid >> 6);
  const int blk = blockIdx.x;
  const int n0 = blk_start(blk), nn = blk < 16 ? 40 : 39;
  const int hh = lane >> 5, l32 = lane & 31;
  const char* Bp = ws + WS_B;

#undef STAMPBASE
#define STAMPBASE 0
  RSTAMP(30); STAMP(0);
  const u32x4 ent = *(const u32x4*)(ws + WS_TBL + (size_t)(blk * NCH + lane) * 16);
  auto load_htile = [&](unsigned dstbase) {
#pragma unroll
    for (int i = 0; i < 3; i++) {
      const int j = wv + 8 * i;
      if (j < 20) {
        const int row = 2 * j + hh;
        const int n = min(n0 + row, NN - 1);
        dma16(h, (unsigned)(n * 512 + ((l32 ^ (row & 15)) << 4)), dstbase + j * 1024);
      }
    }
  };
  load_htile(L_MI);
  unsigned short mypos = 0;
  if (tid < 3 * nn) mypos = ((const unsigned short*)pos)[3 * n0 + tid];
  const u32x4* w1p = (const u32x4*)(ws + WS_W1) + wv * 1024 + lane;
  u32x4 w1f[2][8];
#pragma unroll
  for (int s = 0; s < 8; s++) { w1f[0][s] = w1p[s * 64]; }

  const unsigned o0 = ent.x & 0xffffu, o1 = ent.x >> 16, o2 = ent.y & 0xffffu, o3 = ent.y >> 16, o4 = ent.z & 0xffffu, o5 = ent.z >> 16;
  const unsigned c01 = (o1 - o0) | ((o2 - o1) << 16), c23 = (o3 - o2) | ((o4 - o3) << 16), ct = o5 - o0;
  const unsigned i01 = scan64(c01), i23 = scan64(c23), it = scan64(ct);
  const unsigned total = __builtin_amdgcn_readlane(it, 63);
  const unsigned t01 = __builtin_amdgcn_readlane(i01, 63), t23 = __builtin_amdgcn_readlane(i23, 63);
  const unsigned C0 = t01 & 0xffffu, C1 = t01 >> 16, C2 = t23 & 0xffffu, C3 = t23 >> 16;
  const unsigned E0 = C0, E1 = E0 + C1, E2 = E1 + C2, E3 = E2 + C3;
  const bool fast = total <= MAXT * 32u && C0 >= 32u && C1 >= 32u && C2 >= 32u && C3 >= 32u;
  if (tid < 3 * nn) *LDSP(unsigned short, L_POS + tid * 2u) = mypos;

  STAMP(1);
  if (fast) {
    const int nt = (int)((total + 31u) >> 5);
    {
      const unsigned x01 = i01 - c01, x23 = i23 - c23, xt = it - ct;
      const unsigned p0 = x01 & 0xffffu, p1 = x01 >> 16, p2 = x23 & 0xffffu, p3 = x23 >> 16, p4 = xt - p0 - p1 - p2 - p3;
      if ((lane >> 3) == wv) {
        u32x4 a = {p0 - o0, E0 + p1 - o1, E1 + p2 - o2, E2 + p3 - o3}, b = {E3 + p4 - o4, o0, o5, 0u};
        *LDSP(u32x4, L_PP + lane * 32u) = a;
        *LDSP(u32x4, L_PP + lane * 32u + 16u) = b;
      }
    }
    const unsigned mych = wv * 8u + (lane >> 3);
    const u32x2 rng = *LDSP(u32x2, L_PP + mych * 32u + 20u);
    const u32x2* recg = (const u32x2*)(ws + WS_REC) + mych * CHE;
    u32x2 rc[3];
    bool ok[3];
#pragma unroll
    for (int k = 0; k < 3; k++) {
      const unsigned i = rng.x + (lane & 7) + 8u * k;
      ok[k] = i < rng.y;
      if (ok[k]) rc[k] = recg[i];
    }
#pragma unroll
    for (int s = 0; s < 8; s++) { w1f[1][s] = w1p[512 + s * 64]; }
    const u32x4* smp = (const u32x4*)(ws + WS_SM) + wv * 320 + lane;
    const u32x4 wahi = smp[0], walo = smp[64], wx0 = smp[192], wx1 = smp[256];
    const unsigned wd = smp[128].x;
    unsigned rp[3];
    unsigned long long ps[3];
#pragma unroll
    for (int k = 0; k < 3; k++) {
      if (ok[k]) {
        rp[k] = L_REC + (*LDSP(unsigned, L_PP + mych * 32u + (rc[k].x >> 23) * 4u) + rng.x + (lane & 7) + 8u * k) * 16u;
        ps[k] = load_pos(pos, rc[k].y >> 9);
        *LDSP(u32x2, rp[k]) = rc[k];
      }
    }
    for (unsigned i = rng.x + (lane & 7) + 24u; i < rng.y; i += 8u) {
      const u32x2 r = recg[i];
      const unsigned p = L_REC + (*LDSP(unsigned, L_PP + mych * 32u + (r.x >> 23) * 4u) + i) * 16u;
      u32x4 v; v.x = r.x; v.y = r.y;
      unsigned z, w;
      geom(load_pos(pos, n0 + (r.x >> 23) * 8u + (r.x & 7u)), load_pos(pos, r.y >> 9), z, w);
      v.z = z; v.w = w;
      *LDSP(u32x4, p) = v;
    }
    if (tid < 32 && total + tid < (unsigned)nt * 32u) {
      const u32x4 dm = {7u << 23, 0u, 0u, 0u};
      *LDSP(u32x4, L_REC + (total + tid) * 16u) = dm;
    }
    asm volatile("" :: "v"(w1f[0][0]));
    STAMP(2);
    BAR();

    const unsigned loff0 = (unsigned)((l32 ^ (hh << 3)) << 4);
    const unsigned recrd = L_REC + (unsigned)(4 * wv + hh) * 16u + 4u;
    auto issue_tile = [&](int t, unsigned slot, unsigned s0, unsigned s1) {
      dma16(Bp, s0 | loff0, slot + (2 * wv) * PAIR_PITCH);
      dma16(Bp, s1 | loff0, slot + (2 * wv + 1) * PAIR_PITCH);
      if ((t & 7) == wv) {
        const unsigned x = *LDSP(unsigned, L_REC + (unsigned)(t * 32 + l32) * 16u);
        dma16(eattr, (x & 0x7fffe0u) | (hh << 4), slot + SLOT_B);
      }
    };
#pragma unroll
    for (int t = 0; t < 5; t++) {
      if (t < nt) issue_tile(t, L_RING + t * SLOT_SZ, *LDSP(unsigned, recrd + t * 512u), *LDSP(unsigned, recrd + t * 512u + 32u));
    }
#pragma unroll
    for (int k = 0; k < 3; k++) {
      if (ok[k]) {
        u32x2 v;
        unsigned z, w;
        geom(lds_pos((rc[k].x >> 23) * 8u + (rc[k].x & 7u)), ps[k], z, w);
        v.x = z; v.y = w;
        *LDSP(u32x2, rp[k] + 8u) = v;
      }
    }
    STAMP(3);
    BAR();
    const int T0 = (int)((E0 - 1u) >> 5), T1 = (int)((E1 - 1u) >> 5), T2 = (int)((E2 - 1u) >> 5), T3 = (int)((E3 - 1u) >> 5);
    const unsigned tq = (unsigned)T0 | ((unsigned)T1 << 8) | ((unsigned)T2 << 16) | ((unsigned)T3 << 24);
    auto first_win = [=](int t) { return (unsigned)((t > T0) + (t > T1) + (t > T2) + (t > T3)); };
    if (wv < nt) prep_tile(wv, first_win(wv), true, L_SD + wv * 3072u, lane);
    if (wv >= 4) {
      const int w = wv - 4;
      prep_tile((int)((tq >> (8 * w)) & 0xffu), (unsigned)w + 1u, false, L_POOL + w * 3072u, lane);
    }

    u32x2 aw[6];
    {
      const int r = lane & 15, q = lane >> 4;
      const int prow = 8 * ((r >> 2) & 1) + 4 * (r >> 3) + (r & 3);
#pragma unroll
      for (int T = 0; T < 3; T++) {
        f32x4 a0 = {0.f, 0.f, 0.f, 0.f}, a1 = {0.f, 0.f, 0.f, 0.f};
#pragma unroll
        for (int s = 0; s < 8; s++) {
          const u32x4 hf = *LDSP(u32x4, L_MI + (16 * T + prow) * 512u + (((4 * s + q) ^ prow) << 4));
          a0 = mfma16(hf, w1f[0][s], a0);
          a1 = mfma16(hf, w1f[1][s], a1);
        }
#pragma unroll
        for (int d = 0; d < 2; d++) {
          const unsigned r0 = pk2(a0[2 * d] * NLOG2E, a0[2 * d + 1] * NLOG2E);
          const unsigned r1 = pk2(a1[2 * d] * NLOG2E, a1[2 * d + 1] * NLOG2E);
          const auto sw = __builtin_amdgcn_permlane16_swap(r0, r1, false, false);
          aw[2 * T][d] = sw[0];
          aw[2 * T + 1][d] = sw[1];
        }
      }
    }
    u32x4 acur = {aw[0].x, aw[0].y, wd, 0u}, anext = {aw[1].x, aw[1].y, wd, 0u};
    u32x4 id0, id1;
#pragma unroll
    for (int j = 0; j < 4; j++) {
      const int k = l32 - 8 * hh - 2 * j;
      id0[j] = k == 0 ? 0x3F80u : (k == 1 ? 0x3F800000u : 0u);
      id1[j] = k == 16 ? 0x3F80u : (k == 17 ? 0x3F800000u : 0u);
    }
    const unsigned bread = (unsigned)(l32 >> 1) * PAIR_PITCH + (unsigned)(l32 & 1) * 512u + (unsigned)(((4 * wv + hh) ^ ((l32 & 1) << 3)) << 4);
    const unsigned lread = lane * 16u;
    const f32x16 zero16 = {0.f, 0.f, 0.f, 0.f, 0.f, 0.f, 0.f, 0.f, 0.f, 0.f, 0.f, 0.f, 0.f, 0.f, 0.f, 0.f};
    auto stage1 = [&](unsigned slot, unsigned sd) {
      const u32x4 b0 = *LDSP(u32x4, slot + bread), b1 = *LDSP(u32x4, slot + bread + 32u);
      const u32x4 ef = *LDSP(u32x4, slot + SLOT_B + lread), df = *LDSP(u32x4, sd + lread);
      f32x16 y = mfma32(b0, id0, zero16);
      y = mfma32(b1, id1, y);
      y = mfma32(ef, wahi, y);
      y = mfma32(ef, walo, y);
      y = mfma32(df, acur, y);
      return y;
    };
    int win1 = 0;
    unsigned tq1 = tq;
    auto straddle1 = [&](f32x16& y) {
      const u32x4 d2 = *LDSP(u32x4, L_POOL + win1 * 3072u + lread);
      y = mfma32(d2, anext, y);
      acur.x = anext.x; acur.y = anext.y;
      anext.x = aw[2].x; anext.y = aw[2].y;
      aw[2] = aw[3]; aw[3] = aw[4];
      win1++;
      tq1 = (tq1 >> 8) | 0xff000000u;
    };
    STAMP(4);
    if (nt > 4) WAIT_VM(8); else WAIT_VM(0);
    STAMP(5);
    BAR();
    f32x16 ya = stage1(L_RING, L_SD), yb;
    if ((tq1 & 0xffu) == 0u) straddle1(ya);
    f32x16 acc = zero16;
    int win2 = 0;
    unsigned tq2 = tq;
    unsigned sA = *LDSP(unsigned, recrd + 5 * 512u), sB = *LDSP(unsigned, recrd + 5 * 512u + 32u);
    unsigned slot_cur = L_RING;

    auto iter = [&](int i, const f32x16& y, f32x16& yn) {
      const int rem = nt - 2 - i;
      if (rem >= 3) WAIT_VM(6); else if (rem == 2) WAIT_VM(4); else if (rem == 1) WAIT_VM(2); else WAIT_VM(0);
      BAR();
      if (i + 5 < nt) {
        issue_tile(i + 5, slot_cur, sA, sB);
      }
      if (i == nt - 1) load_htile(L_RING);
      if ((i & 3) == 0 && i >= 4 && ((i >> 2) & 1) == (wv >> 2)) {
        const int t = i + 4 + (wv & 3);
        if (t < nt) prep_tile(t, first_win(t), true, L_SD + (t & 7) * 3072u, lane);
      }
      slot_cur = slot_cur + SLOT_SZ == L_RING + NSLOT * SLOT_SZ ? L_RING : slot_cur + SLOT_SZ;
      const unsigned sd = L_SD + (i & 7) * 3072u, sdn = L_SD + ((i + 1) & 7) * 3072u;
      const u32x4 s0 = *LDSP(u32x4, sd + 1024u + lread), s1 = *LDSP(u32x4, sd + 2048u + lread);
      const int tn = min(i + 6, MAXT - 1);
      sA = *LDSP(unsigned, recrd + tn * 512u);
      sB = *LDSP(unsigned, recrd + tn * 512u + 32u);
      yn = stage1(slot_cur, sdn);
      u32x4 x0, x1;
#pragma unroll
      for (int d = 0; d < 8; d++) {
        f32x2 v; v.x = y[2 * d]; v.y = y[2 * d + 1];
        f32x2 t; t.x = __builtin_amdgcn_exp2f(v.x); t.y = __builtin_amdgcn_exp2f(v.y);
        t = t + 1.0f;
        t.x = __builtin_amdgcn_rcpf(t.x); t.y = __builtin_amdgcn_rcpf(t.y);
        t = t * v;
        const unsigned p = pk2(t.x, t.y);
        if (d < 4) x0[d] = p; else x1[d - 4] = p;
      }
      acc = mfma32(x0, s0, acc);
      acc = mfma32(x1, s1, acc);
      if ((unsigned)i == (tq2 & 0xffu)) {
        flush_window(acc, win2, wv, lane, wx0, wx1);
        const u32x4 q0 = *LDSP(u32x4, L_POOL + win2 * 3072u + 1024u + lread), q1 = *LDSP(u32x4, L_POOL + win2 * 3072u + 2048u + lread);
        acc = mfma32(x0, q0, zero16);
        acc = mfma32(x1, q1, acc);
        win2++;
        tq2 = (tq2 >> 8) | 0xff000000u;
      }
      if ((unsigned)(i + 1) == (tq1 & 0xffu)) straddle1(yn);
    };
    STAMP(6);
#pragma nounroll
    for (int i = 0; i < nt; i += 2) {
      STAMP(12 + (i >> 1));
      iter(i, ya, yb);
      if (i + 1 >= nt) break;
      iter(i + 1, yb, ya);
    }
    STAMP(7);
    flush_window(acc, 4u, wv, lane, wx0, wx1);
  } else {
    float* accm = (float*)smem;
    float* arow = (float*)(smem + 40960);
    float* red = (float*)(smem + 81920);
    float* pacc = (float*)(smem + 81920 + 64);
    WAIT_VM(0);
    __syncthreads();
    const int f = tid & 255;
    for (int r = tid >> 8; r < 40; r += 2) {
      const int n = min(n0 + r, NN - 1);
      float a = 0.f;
      for (int k = 0; k < 256; k++) a += bf2f(h[(size_t)n * 256 + k]) * bf2f(we[(size_t)k * 256 + f]);
      arow[r * 256 + f] = a * NLOG2E;
      accm[r * 256 + f] = 0.f;
    }
    if (tid < 160) pacc[tid] = 0.f;
    float wa[16];
    for (int k = 0; k < 16; k++) wa[k] = bf2f(we[(size_t)(513 + k) * 256 + f]) * NLOG2E;
    const float wdv = bf2f(we[(size_t)512 * 256 + f]) * NLOG2E, wxv = bf2f(wx[f]);
    __syncthreads();
    for (int c = 0; c < NCH; c++) {
      const unsigned b = __builtin_amdgcn_readlane(o0, c), e = __builtin_amdgcn_readlane(o5, c);
      for (unsigned i = b; i < e; i++) {
        const u32x2 r = ((const u32x2*)(ws + WS_REC))[c * CHE + i];
        const unsigned loc = (r.x >> 23) * 8u + (r.x & 7u);
        unsigned z, w;
        geom(lds_pos(loc), load_pos(pos, r.y >> 9), z, w);
        float m = 0.f;
        if (tid < 256) {
          float y = arow[loc * 256 + f] + bf2f(*(const unsigned short*)(Bp + r.y + 2 * f)) + bf2f(z & 0xffffu) * wdv;
          const unsigned short* ea = (const unsigned short*)(eattr + (r.x & 0x7fffe0u));
          for (int k = 0; k < 16; k++) y += bf2f(ea[k]) * wa[k];
          m = rbf(NLN2 * y * __builtin_amdgcn_rcpf(1.0f + __builtin_amdgcn_exp2f(y)));
          accm[loc * 256 + f] += m;
          float cs = m * wxv;
          for (int o = 32; o > 0; o >>= 1) cs += __shfl_xor(cs, o);
          if (lane == 0) red[wv] = cs;
        }
        __syncthreads();
        if (tid < 3) {
          const float cs = red[0] + red[1] + red[2] + red[3];
          const float dv = tid == 0 ? bf2f(z >> 16) : tid == 1 ? bf2f(w & 0xffffu) : bf2f(w >> 16);
          pacc[loc * 4 + 1 + tid] += dv * cs;
        }
        __syncthreads();
      }
    }
    for (int r = tid >> 8; r < 40; r += 2)
      *LDSP(unsigned short, L_MI + r * 512u + (((f >> 3) ^ (r & 15)) << 4) + (f & 7) * 2u) = (unsigned short)pk2(accm[r * 256 + f], 0.f);
    const float pv = tid < 160 ? pacc[tid] : 0.f;
    for (int i = tid; i < 8 * 160; i += 512) *LDSP(float, L_PP + i * 4u) = i < 160 ? pv : 0.f;
    __syncthreads();
    load_htile(L_RING);
  }

  WAIT_VM(0);
  const u32x4* whp = (const u32x4*)(ws + WS_WH) + wv * 2048 + lane;
  u32x4 wf[2][16];
#pragma unroll
  for (int s = 0; s < 16; s++) { wf[0][s] = whp[s * 64]; wf[1][s] = whp[1024 + s * 64]; }
  BAR();
  STAMP(8);
  if (tid < 3 * nn) {
    const int r = tid / 3, d = tid - 3 * r;
    float s = 0.f;
#pragma unroll
    for (int w = 0; w < 8; w++) s += *LDSP(float, L_PP + w * 640u + r * 16u + (d + 1) * 4u);
    out[(size_t)NN * 256 + 3 * n0 + tid] = (unsigned short)pk2(bf2f(*LDSP(unsigned short, L_POS + tid * 2u)) + rbf(s), 0.f);
  }
  {
    const int c = lane & 15, q = lane >> 4;
    f32x4 z[3][2];
#pragma unroll
    for (int T = 0; T < 3; T++) { z[T][0] = f32x4{0.f, 0.f, 0.f, 0.f}; z[T][1] = z[T][0]; }
#pragma unroll
    for (int s = 0; s < 16; s++) {
#pragma unroll
      for (int T = 0; T < 3; T++) {
        const unsigned base = (s < 8 ? L_RING : L_MI) + (16 * T + c) * 512u;
        const u32x4 af = *LDSP(u32x4, base + (((4 * (s & 7) + q) ^ c) << 4));
        z[T][0] = mfma16(wf[0][s], af, z[T][0]);
        z[T][1] = mfma16(wf[1][s], af, z[T][1]);
      }
    }
#pragma unroll
    for (int T = 0; T < 3; T++) {
      const int row = 16 * T + c;
      const u32x4 hv = *LDSP(u32x4, L_RING + row * 512u + (((4 * wv + q) ^ c) << 4));
      u32x4 o;
#pragma unroll
      for (int d = 0; d < 4; d++) {
        const float x0 = z[T][d >> 1][(2 * d) & 3], x1 = z[T][d >> 1][(2 * d + 1) & 3];
        const float g0 = x0 * __builtin_amdgcn_rcpf(1.0f + __builtin_amdgcn_exp2f(x0 * NLOG2E));
        const float g1 = x1 * __builtin_amdgcn_rcpf(1.0f + __builtin_amdgcn_exp2f(x1 * NLOG2E));
        o[d] = pk2(bf2f(hv[d] & 0xffffu) + g0, __uint_as_float(hv[d] & 0xffff0000u) + g1);
      }
      if (row < nn) *(u32x4*)(out + (size_t)(n0 + row) * 256 + wv * 32 + q * 8) = o;
    }
  }
  STAMP(9);
  STAMP(10); RSTAMP(31);
}

extern "C" void kernel_launch(void* const* d_in, const int* in_sizes, int n_in, void* d_out, int out_size, void* d_ws,
                              size_t ws_size, hipStream_t stream) {
  const unsigned short* h = (const unsigned short*)d_in[0];
  const char* pos = (const char*)d_in[1];
  const char* eattr = (const char*)d_in[2];
  const int* src = (const int*)d_in[3];
  const int* dst = (const int*)d_in[4];
  const unsigned short* we = (const unsigned short*)d_in[5];
  const unsigned short* wx = (const unsigned short*)d_in[6];
  const unsigned short* wh = (const unsigned short*)d_in[7];
  (void)hipFuncSetAttribute((const void*)egnn_edge, hipFuncAttributeMaxDynamicSharedMemorySize, (int)L_END);
  egnn_prep<<<NCH + 256, 256, 0, stream>>>(h, src, dst, we, wx, wh, (char*)d_ws);
  egnn_edge<<<NBLK, 512, L_END, stream>>>(h, pos, eattr, we, wx, (const char*)d_ws, (unsigned short*)d_out);
}
```

```cpp
#include <hip/hip_runtime.h>
#include <stdint.h>

typedef short bf16x8 __attribute__((ext_vector_type(8)));
typedef float f32x16 __attribute__((ext_vector_type(16)));
typedef float f32x4 __attribute__((ext_vector_type(4)));
typedef float f32x2 __attribute__((ext_vector_type(2)));
typedef unsigned u32x4 __attribute__((ext_vector_type(4)));
typedef unsigned u32x2 __attribute__((ext_vector_type(2)));
typedef int i32x4 __attribute__((ext_vector_type(4)));
typedef __bf16 bf16v2 __attribute__((ext_vector_type(2)));
#define DI __device__ __forceinline__
#define LDSP(T, off) ((T __attribute__((address_space(3)))*)(uintptr_t)(off))

constexpr int NN = 10000, NE = 160000;
constexpr int NBLK = 256;
constexpr int NCH = 40;
constexpr int CHE = NE / NCH;
constexpr int MAXT = 24;
constexpr float NLOG2E = -1.4426950408889634f;
constexpr float NLN2 = -0.6931471805599453f;

constexpr size_t WS_B = 0;
constexpr size_t WS_REC = WS_B + (size_t)NN * 512;
constexpr size_t WS_TBL = WS_REC + (size_t)NBLK * NCH * 256;
constexpr size_t WS_WH = WS_TBL + (size_t)NBLK * 64 * 32;
constexpr size_t WS_ARR = WS_WH + 262144;
constexpr size_t WS_REL = WS_ARR + 1024;
constexpr unsigned NREL = 8;
constexpr int CELL = 48;

constexpr unsigned PAIR_PITCH = 1040;
constexpr unsigned SLOT_SZ = 16 * PAIR_PITCH;
constexpr unsigned L_RING = 0;
constexpr unsigned L_SD = L_RING + 4 * SLOT_SZ;
constexpr unsigned L_PP = L_SD + 8 * 4096;
constexpr unsigned L_POOL = L_PP + 2048;
constexpr unsigned MIP = 528;
constexpr unsigned L_MI = L_POOL + 5 * 2048;
constexpr unsigned L_REC = L_MI + 42 * MIP;
constexpr unsigned L_PE = L_REC + MAXT * 32 * 8;
constexpr unsigned L_POS = 160768;
constexpr unsigned L_END = L_POS + 1024;
static_assert(L_END <= 163840, "LDS");

DI float bf2f(unsigned b) { return __uint_as_float(b << 16); }
DI unsigned pk2(float a, float b) {
  f32x2 v; v.x = a; v.y = b;
  return __builtin_bit_cast(unsigned, __builtin_convertvector(v, bf16v2));
}
DI float rbf(float a) { return __uint_as_float(pk2(a, 0.f) << 16); }
DI bf16x8 asb(u32x4 v) { return __builtin_bit_cast(bf16x8, v); }
DI f32x16 mfma32(u32x4 a, u32x4 b, f32x16 c) { return __builtin_amdgcn_mfma_f32_32x32x16_bf16(asb(a), asb(b), c, 0, 0, 0); }
DI f32x4 mfma16(u32x4 a, u32x4 b, f32x4 c) { return __builtin_amdgcn_mfma_f32_16x16x32_bf16(asb(a), asb(b), c, 0, 0, 0); }
typedef short s16x4 __attribute__((ext_vector_type(4)));
DI u32x2 trread(unsigned a) { return __builtin_bit_cast(u32x2, __builtin_amdgcn_ds_read_tr16_b64_v4i16(LDSP(s16x4, a))); }
DI void st16(void* p, u32x4 v) { asm volatile("global_store_dwordx4 %0, %1, off sc1\n\ts_nop 1" :: "v"(p), "v"(v) : "memory"); }

DI void st4(void* p, unsigned v) { asm volatile("global_store_dword %0, %1, off sc1" :: "v"(p), "v"(v) : "memory"); }
DI void st16s(const void* base, unsigned voff, u32x4 v) { asm volatile("global_store_dwordx4 %0, %1, %2 sc1\n\ts_nop 1" :: "v"(voff), "v"(v), "s"(base) : "memory"); }

DI void dma16(const void* base, unsigned voff, unsigned lds) {
  asm volatile("s_mov_b32 m0, %2\n\ts_nop 0\n\tglobal_load_lds_dwordx4 %0, %1"
               :: "v"(voff), "s"(base), "s"(__builtin_amdgcn_readfirstlane(lds)) : "memory");
}
template <int GS, int LS>
DI void dma16x4(const void* base, unsigned voff, unsigned lds) {
  asm volatile("s_mov_b32 m0, %2\n\ts_mov_b64 s[34:35], %1\n\tglobal_load_lds_dwordx4 %0, s[34:35]\n\t"
               "s_add_u32 m0, m0, %4\n\ts_add_u32 s34, s34, %3\n\ts_addc_u32 s35, s35, 0\n\tglobal_load_lds_dwordx4 %0, s[34:35]\n\t"
               "s_add_u32 m0, m0, %4\n\ts_add_u32 s34, s34, %3\n\ts_addc_u32 s35, s35, 0\n\tglobal_load_lds_dwordx4 %0, s[34:35]\n\t"
               "s_add_u32 m0, m0, %4\n\ts_add_u32 s34, s34, %3\n\ts_addc_u32 s35, s35, 0\n\tglobal_load_lds_dwordx4 %0, s[34:35]"
               :: "v"(voff), "s"(base), "s"(__builtin_amdgcn_readfirstlane(lds)), "i"(GS), "i"(LS) : "s34", "s35", "scc", "memory");
}
DI void dma16c(const void* base, unsigned voff, unsigned lds) {
  asm volatile("s_mov_b32 m0, %2\n\ts_nop 0\n\tglobal_load_lds_dwordx4 %0, %1 sc1"
               :: "v"(voff), "s"(base), "s"(__builtin_amdgcn_readfirstlane(lds)) : "memory");
}
#define GLD(op, dst, voff, base) asm volatile(op " %0, %1, %2" : "=v"(dst) : "v"(voff), "s"(base) : "memory")
#define GLD16(dst, voff, base) GLD("global_load_dwordx4", dst, voff, base)
#define GLDC(op, dst, voff, base) asm volatile(op " %0, %1, %2 sc1" : "=v"(dst) : "v"(voff), "s"(base) : "memory")
#define WAIT_VM(n) asm volatile("s_waitcnt vmcnt(" #n ")" ::: "memory")
#define WAIT_LGKM0() asm volatile("s_waitcnt lgkmcnt(0)" ::: "memory")
#define BAR() do { asm volatile("s_waitcnt lgkmcnt(0)" ::: "memory"); __builtin_amdgcn_s_barrier(); asm volatile("" ::: "memory"); } while (0)

#define ABL 0
#define STAMP_DECL
#define STAMP_PIN
#define STAMP(k)
#define STAMP_OUT(base)
DI unsigned scan64(unsigned v) {
  v += __builtin_amdgcn_update_dpp(0u, v, 0x111, 0xf, 0xf, false);
  v += __builtin_amdgcn_update_dpp(0u, v, 0x112, 0xf, 0xf, false);
  v += __builtin_amdgcn_update_dpp(0u, v, 0x114, 0xf, 0xf, false);
  v += __builtin_amdgcn_update_dpp(0u, v, 0x118, 0xf, 0xf, false);
  v += __builtin_amdgcn_update_dpp(0u, v, 0x142, 0xa, 0xf, false);
  v += __builtin_amdgcn_update_dpp(0u, v, 0x143, 0xc, 0xf, false);
  return v;
}

DI void tr8x2(const unsigned (&w)[8], u32x4& f0, u32x4& f1) {
#pragma unroll
  for (int j = 0; j < 4; j++) {
    f0[j] = __builtin_amdgcn_perm(w[2 * j + 1], w[2 * j], 0x05040100u);
    f1[j] = __builtin_amdgcn_perm(w[2 * j + 1], w[2 * j], 0x07060302u);
  }
}

constexpr int GM = 96, GT = (NN + GM - 1) / GM;
static_assert(WS_REC == 5120000 && WS_TBL == 7741440 && WS_WH == 8265728 && WS_ARR == 8527872 && WS_REL == 8528896, "offsets used by the assembly");

typedef unsigned u32x16 __attribute__((ext_vector_type(16)));
typedef unsigned u32x8 __attribute__((ext_vector_type(8)));
typedef unsigned u32x32 __attribute__((ext_vector_type(32)));
typedef unsigned short u16x2 __attribute__((ext_vector_type(2)));

DI void geom(unsigned dlo, unsigned dhi, unsigned slo, unsigned shi, unsigned& xy, unsigned& zd) {
  xy = pk2(__uint_as_float(dlo << 16) - __uint_as_float(slo << 16), __uint_as_float(dlo & 0xffff0000u) - __uint_as_float(slo & 0xffff0000u));
  const unsigned z0 = pk2(__uint_as_float(dhi << 16) - __uint_as_float(shi << 16), 0.f);
  const float dx = __uint_as_float(xy << 16), dy = __uint_as_float(xy & 0xffff0000u), dz = __uint_as_float(z0 << 16);
  const unsigned q = pk2(dx * dx, dy * dy);
  const float d2 = __uint_as_float(q << 16) + __uint_as_float(q & 0xffff0000u) + rbf(dz * dz);
  zd = pk2(dz, d2);
}
DI void unshift(u32x2 v, unsigned n, unsigned& lo, unsigned& hi) {
  const unsigned sh = (n & 1u) << 4;
  lo = __builtin_amdgcn_alignbit(v.y, v.x, sh);
  hi = v.y >> sh;
}
#define PHASE1_ASM \
  "v_lshrrev_b32 v1, 4, v111\n\t" \
  "v_and_b32 v2, 15, v1\n\t" \
  "v_lshrrev_b32 v3, 4, v1\n\t" \
  "v_lshrrev_b32 v4, 5, v1\n\t" \
  "v_and_b32 v5, 31, v1\n\t" \
  "s_and_b32 s63, s53, 3\n\t" \
  "v_lshl_or_b32 v14, v3, 2, s63\n\t" \
  "v_xor_b32 v14, v14, v2\n\t" \
  "s_lshl_b32 s62, s53, 11\n\t" \
  "v_lshl_add_u32 v6, v3, 9, s62\n\t" \
  "v_lshl_add_u32 v6, v14, 4, v6\n\t" \
  "s_lshl_b32 s62, s53, 10\n\t" \
  "s_lshl_b32 s63, s53, 1\n\t" \
  "v_add_u32 v15, s63, v4\n\t" \
  "v_and_b32 v16, 15, v15\n\t" \
  "v_xor_b32 v16, v16, v5\n\t" \
  "v_lshlrev_b32 v16, 4, v16\n\t" \
  "s_cmp_eq_u32 s54, 0\n\t" \
  "s_cbranch_scc0 NOTG_%=\n\t" \
  "s_mul_i32 s63, s55, 96\n\t" \
  "v_add_u32 v20, s63, v15\n\t" \
  "v_lshl_add_u32 v20, v20, 9, v16\n\t" \
  "v_add_u32 v21, 5119488, v16\n\t" \
  "v_add_u32 v22, 0, v20\n\t" \
  "s_add_u32 m0, s62, 16384\n\t" \
  "v_min_u32 v22, v22, v21\n\t" \
  "global_load_lds_dwordx4 v22, s[36:37]\n\t" \
  "v_add_u32 v22, 8192, v20\n\t" \
  "s_add_u32 m0, s62, 24576\n\t" \
  "v_min_u32 v22, v22, v21\n\t" \
  "global_load_lds_dwordx4 v22, s[36:37]\n\t" \
  "s_lshl_b32 s63, s56, 8\n\t" \
  "v_add_u32 v6, s63, v6\n\t" \
  "s_add_u32 m0, s62, 65536\n\t" \
  "s_add_u32 s64, s38, 131072\n\t" \
  "s_addc_u32 s65, s39, 0\n\t" \
  "global_load_lds_dwordx4 v6, s[64:65]\n\t" \
  "s_add_u32 m0, s62, 73728\n\t" \
  "s_add_u32 s64, s38, 147456\n\t" \
  "s_addc_u32 s65, s39, 0\n\t" \
  "global_load_lds_dwordx4 v6, s[64:65]\n\t" \
  "s_add_u32 m0, s62, 81920\n\t" \
  "s_add_u32 s64, s38, 163840\n\t" \
  "s_addc_u32 s65, s39, 0\n\t" \
  "global_load_lds_dwordx4 v6, s[64:65]\n\t" \
  "s_add_u32 m0, s62, 90112\n\t" \
  "s_add_u32 s64, s38, 180224\n\t" \
  "s_addc_u32 s65, s39, 0\n\t" \
  "global_load_lds_dwordx4 v6, s[64:65]\n\t" \
  "s_add_u32 m0, s62, 98304\n\t" \
  "s_add_u32 s64, s38, 196608\n\t" \
  "s_addc_u32 s65, s39, 0\n\t" \
  "global_load_lds_dwordx4 v6, s[64:65]\n\t" \
  "s_add_u32 m0, s62, 106496\n\t" \
  "s_add_u32 s64, s38, 212992\n\t" \
  "s_addc_u32 s65, s39, 0\n\t" \
  "global_load_lds_dwordx4 v6, s[64:65]\n\t" \
  "s_add_u32 m0, s62, 114688\n\t" \
  "s_add_u32 s64, s38, 229376\n\t" \
  "s_addc_u32 s65, s39, 0\n\t" \
  "global_load_lds_dwordx4 v6, s[64:65]\n\t" \
  "s_add_u32 m0, s62, 122880\n\t" \
  "s_add_u32 s64, s38, 245760\n\t" \
  "s_addc_u32 s65, s39, 0\n\t" \
  "global_load_lds_dwordx4 v6, s[64:65]\n\t" \
  "v_subrev_u32 v6, s63, v6\n\t" \
  "s_barrier\n\t" \
  "v_add_u32 v22, 16384, v20\n\t" \
  "s_add_u32 m0, s62, 32768\n\t" \
  "v_min_u32 v22, v22, v21\n\t" \
  "global_load_lds_dwordx4 v22, s[36:37]\n\t" \
  "v_add_u32 v22, 24576, v20\n\t" \
  "s_add_u32 m0, s62, 40960\n\t" \
  "v_min_u32 v22, v22, v21\n\t" \
  "global_load_lds_dwordx4 v22, s[36:37]\n\t" \
  "v_add_u32 v22, 32768, v20\n\t" \
  "s_add_u32 m0, s62, 49152\n\t" \
  "v_min_u32 v22, v22, v21\n\t" \
  "global_load_lds_dwordx4 v22, s[36:37]\n\t" \
  "v_add_u32 v22, 40960, v20\n\t" \
  "s_add_u32 m0, s62, 57344\n\t" \
  "v_min_u32 v22, v22, v21\n\t" \
  "global_load_lds_dwordx4 v22, s[36:37]\n\t" \
  "s_cmp_eq_u32 s53, 7\n\t" \
  "s_cselect_b32 s54, s55, 64\n\t" \
  "s_cmp_lt_u32 s54, 64\n\t" \
  "s_cbranch_scc0 NP1_%=\n\t" \
  "s_lshl_b32 s63, s55, 4\n\t" \
  "s_add_u32 s63, s63, s56\n\t" \
  "s_lshl_b32 s63, s63, 8\n\t" \
  "v_lshl_add_u32 v19, v1, 2, s63\n\t" \
  "global_load_dword v244, v19, s[40:41] offset:0\n\t" \
  "global_load_dword v245, v19, s[40:41] offset:512\n\t" \
  "global_load_dword v246, v19, s[40:41] offset:1024\n\t" \
  "global_load_dword v247, v19, s[40:41] offset:1536\n\t" \
  "global_load_dword v248, v19, s[40:41] offset:2048\n\t" \
  "global_load_dword v249, v19, s[40:41] offset:2560\n\t" \
  "global_load_dword v250, v19, s[40:41] offset:3072\n\t" \
  "global_load_dword v251, v19, s[40:41] offset:3584\n\t" \
  "NP1_%=:\n\t" \
  "v_lshrrev_b32 v20, 2, v2\n\t" \
  "v_lshl_add_u32 v21, v3, 3, v20\n\t" \
  "v_lshlrev_b32 v21, 8, v21\n\t" \
  "v_and_b32 v22, 1, v2\n\t" \
  "v_lshl_add_u32 v21, v22, 3, v21\n\t" \
  "v_bfe_u32 v22, v2, 1, 1\n\t" \
  "s_and_b32 s63, s53, 3\n\t" \
  "s_lshl_b32 s63, s63, 2\n\t" \
  "v_or_b32 v22, s63, v22\n\t" \
  "v_lshlrev_b32 v20, 2, v20\n\t" \
  "v_xor_b32 v22, v22, v20\n\t" \
  "v_and_b32 v20, 1, v3\n\t" \
  "v_lshlrev_b32 v20, 1, v20\n\t" \
  "v_xor_b32 v22, v22, v20\n\t" \
  "v_lshl_add_u32 v7, v22, 4, v21\n\t" \
  "v_xor_b32 v8, 16, v7\n\t" \
  "v_add_u32 v8, 1024, v8\n\t" \
  "v_add_u32 v88, 65536, v7\n\t" \
  "v_add_u32 v89, 65536, v8\n\t" \
  "v_xor_b32 v90, 32, v88\n\t" \
  "v_xor_b32 v91, 32, v89\n\t" \
  "s_lshr_b32 s63, s53, 2\n\t" \
  "s_lshl_b32 s63, s63, 4\n\t" \
  "v_add_u32 v23, s63, v2\n\t" \
  "v_xor_b32 v92, v3, v2\n\t" \
  "v_lshlrev_b32 v92, 4, v92\n\t" \
  "v_lshl_add_u32 v92, v23, 9, v92\n\t" \
  "v_add_u32 v92, 16384, v92\n\t" \
  "v_xor_b32 v93, 64, v92\n\t" \
  "v_xor_b32 v94, 128, v92\n\t" \
  "v_xor_b32 v95, 192, v92\n\t" \
  "v_xor_b32 v96, 256, v92\n\t" \
  "v_xor_b32 v97, 320, v92\n\t" \
  "v_xor_b32 v98, 384, v92\n\t" \
  "v_xor_b32 v99, 448, v92\n\t" \
  "s_mul_i32 s63, s55, 96\n\t" \
  "v_add_u32 v23, s63, v23\n\t" \
  "v_and_b32 v20, 1, v3\n\t" \
  "v_lshrrev_b32 v21, 1, v3\n\t" \
  "v_lshlrev_b32 v21, 4, v21\n\t" \
  "v_lshl_add_u32 v21, v20, 5, v21\n\t" \
  "v_lshl_add_u32 v23, v23, 9, v21\n\t" \
  "v_add_u32 v21, 5119488, v21\n\t" \
  "s_lshl_b32 s63, s56, 2\n\t" \
  "s_and_b32 s66, s53, 3\n\t" \
  "s_add_u32 s63, s63, s66\n\t" \
  "s_lshl_b32 s63, s63, 6\n\t" \
  "s_add_u32 s66, s42, s63\n\t" \
  "s_addc_u32 s67, s43, 0\n\t" \
  "s_mov_b32 s68, 0xbfb8aa3b\n\t" \
  "s_mov_b32 s69, 0xbfb8aa3b\n\t" \
  "s_cmp_lt_u32 s54, 64\n\t" \
  "s_cbranch_scc1 W8a_%=\n\t" \
  "s_waitcnt vmcnt(4)\n\t" \
  "W8a_%=:\n\t" \
  "s_waitcnt vmcnt(12)\n\t" \
  "s_barrier\n\t" \
  "ds_read_b64_tr_b16 v[144:145], v88 offset:0\n\t" \
  "ds_read_b64_tr_b16 v[146:147], v89 offset:0\n\t" \
  "ds_read_b64_tr_b16 v[148:149], v90 offset:0\n\t" \
  "ds_read_b64_tr_b16 v[150:151], v91 offset:0\n\t" \
  "ds_read_b128 v[24:27], v92\n\t" \
  "ds_read_b64_tr_b16 v[152:153], v88 offset:8192\n\t" \
  "ds_read_b64_tr_b16 v[154:155], v89 offset:8192\n\t" \
  "ds_read_b64_tr_b16 v[156:157], v90 offset:8192\n\t" \
  "ds_read_b64_tr_b16 v[158:159], v91 offset:8192\n\t" \
  "ds_read_b128 v[28:31], v93\n\t" \
  "ds_read_b64_tr_b16 v[160:161], v88 offset:16384\n\t" \
  "ds_read_b64_tr_b16 v[162:163], v89 offset:16384\n\t" \
  "ds_read_b64_tr_b16 v[164:165], v90 offset:16384\n\t" \
  "ds_read_b64_tr_b16 v[166:167], v91 offset:16384\n\t" \
  "ds_read_b128 v[32:35], v94\n\t" \
  "ds_read_b64_tr_b16 v[168:169], v88 offset:24576\n\t" \
  "ds_read_b64_tr_b16 v[170:171], v89 offset:24576\n\t" \
  "ds_read_b64_tr_b16 v[172:173], v90 offset:24576\n\t" \
  "ds_read_b64_tr_b16 v[174:175], v91 offset:24576\n\t" \
  "ds_read_b128 v[36:39], v95\n\t" \
  "ds_read_b64_tr_b16 v[176:177], v88 offset:32768\n\t" \
  "ds_read_b64_tr_b16 v[178:179], v89 offset:32768\n\t" \
  "ds_read_b64_tr_b16 v[180:181], v90 offset:32768\n\t" \
  "ds_read_b64_tr_b16 v[182:183], v91 offset:32768\n\t" \
  "ds_read_b128 v[40:43], v96\n\t" \
  "ds_read_b64_tr_b16 v[184:185], v88 offset:40960\n\t" \
  "ds_read_b64_tr_b16 v[186:187], v89 offset:40960\n\t" \
  "ds_read_b64_tr_b16 v[188:189], v90 offset:40960\n\t" \
  "ds_read_b64_tr_b16 v[190:191], v91 offset:40960\n\t" \
  "ds_read_b128 v[44:47], v97\n\t" \
  "ds_read_b64_tr_b16 v[192:193], v88 offset:49152\n\t" \
  "ds_read_b64_tr_b16 v[194:195], v89 offset:49152\n\t" \
  "ds_read_b64_tr_b16 v[196:197], v90 offset:49152\n\t" \
  "ds_read_b64_tr_b16 v[198:199], v91 offset:49152\n\t" \
  "ds_read_b128 v[48:51], v98\n\t" \
  "ds_read_b64_tr_b16 v[200:201], v88 offset:57344\n\t" \
  "ds_read_b64_tr_b16 v[202:203], v89 offset:57344\n\t" \
  "ds_read_b64_tr_b16 v[204:205], v90 offset:57344\n\t" \
  "ds_read_b64_tr_b16 v[206:207], v91 offset:57344\n\t" \
  "ds_read_b128 v[52:55], v99\n\t" \
  "s_waitcnt lgkmcnt(15)\n\t" \
  "v_mfma_f32_16x16x32_bf16 v[112:115], v[144:147], v[24:27], 0\n\t" \
  "v_mfma_f32_16x16x32_bf16 v[116:119], v[148:151], v[24:27], 0\n\t" \
  "s_waitcnt lgkmcnt(15)\n\t" \
  "v_mfma_f32_16x16x32_bf16 v[112:115], v[152:155], v[28:31], v[112:115]\n\t" \
  "v_lshl_add_u32 v17, v15, 8, s52\n\t" \
  "v_mfma_f32_16x16x32_bf16 v[116:119], v[156:159], v[28:31], v[116:119]\n\t" \
  "v_lshl_add_u32 v18, v17, 9, v16\n\t" \
  "s_waitcnt lgkmcnt(15)\n\t" \
  "v_mfma_f32_16x16x32_bf16 v[112:115], v[160:163], v[32:35], v[112:115]\n\t" \
  "s_add_u32 m0, s62, 131072\n\t" \
  "s_nop 0\n\t" \
  "global_load_lds_dwordx4 v18, s[36:37]\n\t" \
  "v_mfma_f32_16x16x32_bf16 v[116:119], v[164:167], v[32:35], v[116:119]\n\t" \
  "v_add_u32 v18, 2097152, v18\n\t" \
  "s_waitcnt lgkmcnt(15)\n\t" \
  "v_mfma_f32_16x16x32_bf16 v[112:115], v[168:171], v[36:39], v[112:115]\n\t" \
  "s_add_u32 m0, s62, 139264\n\t" \
  "s_nop 0\n\t" \
  "global_load_lds_dwordx4 v18, s[36:37]\n\t" \
  "v_mfma_f32_16x16x32_bf16 v[116:119], v[172:175], v[36:39], v[116:119]\n\t" \
  "s_add_u32 s63, s62, 262144\n\t" \
  "v_add_u32 v19, s63, v111\n\t" \
  "s_waitcnt lgkmcnt(15)\n\t" \
  "v_mfma_f32_16x16x32_bf16 v[112:115], v[176:179], v[40:43], v[112:115]\n\t" \
  "s_add_u32 m0, s62, 151552\n\t" \
  "s_nop 0\n\t" \
  "global_load_lds_dwordx4 v19, s[38:39]\n\t" \
  "v_mfma_f32_16x16x32_bf16 v[116:119], v[180:183], v[40:43], v[116:119]\n\t" \
  "v_add_u32 v17, 8192, v17\n\t" \
  "s_waitcnt lgkmcnt(10)\n\t" \
  "v_mfma_f32_16x16x32_bf16 v[112:115], v[184:187], v[44:47], v[112:115]\n\t" \
  "v_min_u32 v17, 9999, v17\n\t" \
  "v_mfma_f32_16x16x32_bf16 v[116:119], v[188:191], v[44:47], v[116:119]\n\t" \
  "v_lshl_add_u32 v17, v17, 9, v16\n\t" \
  "s_waitcnt lgkmcnt(5)\n\t" \
  "v_mfma_f32_16x16x32_bf16 v[112:115], v[192:195], v[48:51], v[112:115]\n\t" \
  "v_mfma_f32_16x16x32_bf16 v[116:119], v[196:199], v[48:51], v[116:119]\n\t" \
  "s_waitcnt lgkmcnt(0)\n\t" \
  "v_mfma_f32_16x16x32_bf16 v[112:115], v[200:203], v[52:55], v[112:115]\n\t" \
  "v_mfma_f32_16x16x32_bf16 v[116:119], v[204:207], v[52:55], v[116:119]\n\t" \
  "s_cmp_lt_u32 s54, 64\n\t" \
  "s_cbranch_scc1 W8b_%=\n\t" \
  "s_waitcnt vmcnt(3)\n\t" \
  "W8b_%=:\n\t" \
  "s_waitcnt vmcnt(11)\n\t" \
  "s_barrier\n\t" \
  "ds_read_b128 v[56:59], v92 offset:16384\n\t" \
  "ds_read_b128 v[60:63], v93 offset:16384\n\t" \
  "ds_read_b128 v[64:67], v94 offset:16384\n\t" \
  "ds_read_b128 v[68:71], v95 offset:16384\n\t" \
  "ds_read_b128 v[72:75], v96 offset:16384\n\t" \
  "ds_read_b128 v[76:79], v97 offset:16384\n\t" \
  "ds_read_b128 v[80:83], v98 offset:16384\n\t" \
  "ds_read_b128 v[84:87], v99 offset:16384\n\t" \
  "ds_read_b128 v[208:211], v92 offset:32768\n\t" \
  "ds_read_b128 v[212:215], v93 offset:32768\n\t" \
  "ds_read_b128 v[216:219], v94 offset:32768\n\t" \
  "ds_read_b128 v[220:223], v95 offset:32768\n\t" \
  "ds_read_b128 v[224:227], v96 offset:32768\n\t" \
  "ds_read_b128 v[228:231], v97 offset:32768\n\t" \
  "ds_read_b128 v[232:235], v98 offset:32768\n\t" \
  "ds_read_b128 v[236:239], v99 offset:32768\n\t" \
  "s_cmp_lt_u32 s53, 4\n\t" \
  "s_cbranch_scc0 XAg_%=\n\t" \
  "s_add_u32 m0, s62, 147456\n\t" \
  "s_nop 0\n\t" \
  "global_load_lds_dwordx4 v17, s[36:37]\n\t" \
  "XAg_%=:\n\t" \
  "s_cmp_eq_u32 s53, 4\n\t" \
  "s_cbranch_scc0 XBg_%=\n\t" \
  "s_mov_b32 exec_hi, 0\n\t" \
  "v_add_u32 v19, 270336, v111\n\t" \
  "s_mov_b32 m0, 159744\n\t" \
  "s_nop 0\n\t" \
  "global_load_lds_dwordx4 v19, s[38:39]\n\t" \
  "s_mov_b32 m0, 160256\n\t" \
  "s_nop 0\n\t" \
  "global_load_lds_dwordx4 v111, s[50:51]\n\t" \
  "s_mov_b32 exec_hi, -1\n\t" \
  "XBg_%=:\n\t" \
  "s_cmp_eq_u32 s53, 5\n\t" \
  "s_cbranch_scc0 NEP_%=\n\t" \
  "s_lshl1_add_u32 s63, s55, s56\n\t" \
  "s_mul_i32 s63, s63, 24576\n\t" \
  "v_lshl_add_u32 v19, v1, 7, s63\n\t" \
  "v_min_u32 v18, 5119996, v19\n\t" \
  "global_load_dword v244, v18, s[72:73]\n\t" \
  "v_add_u32 v19, 8192, v19\n\t" \
  "v_min_u32 v18, 5119996, v19\n\t" \
  "global_load_dword v245, v18, s[72:73]\n\t" \
  "v_add_u32 v19, 8192, v19\n\t" \
  "v_min_u32 v18, 5119996, v19\n\t" \
  "global_load_dword v246, v18, s[72:73]\n\t" \
  "NEP_%=:\n\t" \
  "s_waitcnt lgkmcnt(15)\n\t" \
  "v_mfma_f32_16x16x32_bf16 v[120:123], v[144:147], v[56:59], 0\n\t" \
  "v_mfma_f32_16x16x32_bf16 v[124:127], v[148:151], v[56:59], 0\n\t" \
  "v_pk_mul_f32 v[112:113], v[112:113], s[68:69]\n\t" \
  "s_waitcnt lgkmcnt(14)\n\t" \
  "v_mfma_f32_16x16x32_bf16 v[120:123], v[152:155], v[60:63], v[120:123]\n\t" \
  "v_pk_mul_f32 v[114:115], v[114:115], s[68:69]\n\t" \
  "v_mfma_f32_16x16x32_bf16 v[124:127], v[156:159], v[60:63], v[124:127]\n\t" \
  "v_pk_mul_f32 v[116:117], v[116:117], s[68:69]\n\t" \
  "s_waitcnt lgkmcnt(13)\n\t" \
  "v_mfma_f32_16x16x32_bf16 v[120:123], v[160:163], v[64:67], v[120:123]\n\t" \
  "v_pk_mul_f32 v[118:119], v[118:119], s[68:69]\n\t" \
  "v_mfma_f32_16x16x32_bf16 v[124:127], v[164:167], v[64:67], v[124:127]\n\t" \
  "v_cvt_pk_bf16_f32 v100, v112, v113\n\t" \
  "s_waitcnt lgkmcnt(12)\n\t" \
  "v_mfma_f32_16x16x32_bf16 v[120:123], v[168:171], v[68:71], v[120:123]\n\t" \
  "v_cvt_pk_bf16_f32 v102, v116, v117\n\t" \
  "v_mfma_f32_16x16x32_bf16 v[124:127], v[172:175], v[68:71], v[124:127]\n\t" \
  "v_cvt_pk_bf16_f32 v101, v114, v115\n\t" \
  "s_waitcnt lgkmcnt(11)\n\t" \
  "v_mfma_f32_16x16x32_bf16 v[120:123], v[176:179], v[72:75], v[120:123]\n\t" \
  "v_cvt_pk_bf16_f32 v103, v118, v119\n\t" \
  "v_mfma_f32_16x16x32_bf16 v[124:127], v[180:183], v[72:75], v[124:127]\n\t" \
  "v_add_u32 v20, 0, v23\n\t" \
  "s_waitcnt lgkmcnt(10)\n\t" \
  "v_mfma_f32_16x16x32_bf16 v[120:123], v[184:187], v[76:79], v[120:123]\n\t" \
  "v_min_u32 v20, v20, v21\n\t" \
  "v_mfma_f32_16x16x32_bf16 v[124:127], v[188:191], v[76:79], v[124:127]\n\t" \
  "v_permlane16_swap_b32 v100, v102\n\t" \
  "s_waitcnt lgkmcnt(9)\n\t" \
  "v_mfma_f32_16x16x32_bf16 v[120:123], v[192:195], v[80:83], v[120:123]\n\t" \
  "v_permlane16_swap_b32 v101, v103\n\t" \
  "v_mfma_f32_16x16x32_bf16 v[124:127], v[196:199], v[80:83], v[124:127]\n\t" \
  "s_nop 0\n\t" \
  "global_store_dwordx4 v20, v[100:103], s[66:67] sc1\n\t" \
  "s_waitcnt lgkmcnt(8)\n\t" \
  "v_mfma_f32_16x16x32_bf16 v[120:123], v[200:203], v[84:87], v[120:123]\n\t" \
  "v_mfma_f32_16x16x32_bf16 v[124:127], v[204:207], v[84:87], v[124:127]\n\t" \
  "s_waitcnt lgkmcnt(7)\n\t" \
  "v_mfma_f32_16x16x32_bf16 v[112:115], v[144:147], v[208:211], 0\n\t" \
  "v_mfma_f32_16x16x32_bf16 v[116:119], v[148:151], v[208:211], 0\n\t" \
  "s_waitcnt lgkmcnt(6)\n\t" \
  "v_mfma_f32_16x16x32_bf16 v[112:115], v[152:155], v[212:215], v[112:115]\n\t" \
  "v_mfma_f32_16x16x32_bf16 v[116:119], v[156:159], v[212:215], v[116:119]\n\t" \
  "s_waitcnt lgkmcnt(5)\n\t" \
  "v_mfma_f32_16x16x32_bf16 v[112:115], v[160:163], v[216:219], v[112:115]\n\t" \
  "v_pk_mul_f32 v[120:121], v[120:121], s[68:69]\n\t" \
  "v_mfma_f32_16x16x32_bf16 v[116:119], v[164:167], v[216:219], v[116:119]\n\t" \
  "v_pk_mul_f32 v[122:123], v[122:123], s[68:69]\n\t" \
  "s_waitcnt lgkmcnt(4)\n\t" \
  "v_mfma_f32_16x16x32_bf16 v[112:115], v[168:171], v[220:223], v[112:115]\n\t" \
  "v_pk_mul_f32 v[124:125], v[124:125], s[68:69]\n\t" \
  "v_mfma_f32_16x16x32_bf16 v[116:119], v[172:175], v[220:223], v[116:119]\n\t" \
  "v_pk_mul_f32 v[126:127], v[126:127], s[68:69]\n\t" \
  "s_waitcnt lgkmcnt(3)\n\t" \
  "v_mfma_f32_16x16x32_bf16 v[112:115], v[176:179], v[224:227], v[112:115]\n\t" \
  "v_cvt_pk_bf16_f32 v104, v120, v121\n\t" \
  "v_mfma_f32_16x16x32_bf16 v[116:119], v[180:183], v[224:227], v[116:119]\n\t" \
  "v_cvt_pk_bf16_f32 v106, v124, v125\n\t" \
  "s_waitcnt lgkmcnt(2)\n\t" \
  "v_mfma_f32_16x16x32_bf16 v[112:115], v[184:187], v[228:231], v[112:115]\n\t" \
  "v_cvt_pk_bf16_f32 v105, v122, v123\n\t" \
  "v_mfma_f32_16x16x32_bf16 v[116:119], v[188:191], v[228:231], v[116:119]\n\t" \
  "v_cvt_pk_bf16_f32 v107, v126, v127\n\t" \
  "s_waitcnt lgkmcnt(1)\n\t" \
  "v_mfma_f32_16x16x32_bf16 v[112:115], v[192:195], v[232:235], v[112:115]\n\t" \
  "v_add_u32 v20, 16384, v23\n\t" \
  "v_mfma_f32_16x16x32_bf16 v[116:119], v[196:199], v[232:235], v[116:119]\n\t" \
  "v_min_u32 v20, v20, v21\n\t" \
  "s_waitcnt lgkmcnt(0)\n\t" \
  "v_mfma_f32_16x16x32_bf16 v[112:115], v[200:203], v[236:239], v[112:115]\n\t" \
  "v_permlane16_swap_b32 v104, v106\n\t" \
  "v_mfma_f32_16x16x32_bf16 v[116:119], v[204:207], v[236:239], v[116:119]\n\t" \
  "v_permlane16_swap_b32 v105, v107\n\t" \
  "s_nop 0\n\t" \
  "global_store_dwordx4 v20, v[104:107], s[66:67] sc1\n\t" \
  "s_nop 7\n\t" \
  "v_pk_mul_f32 v[112:113], v[112:113], s[68:69]\n\t" \
  "v_pk_mul_f32 v[114:115], v[114:115], s[68:69]\n\t" \
  "v_pk_mul_f32 v[116:117], v[116:117], s[68:69]\n\t" \
  "v_pk_mul_f32 v[118:119], v[118:119], s[68:69]\n\t" \
  "v_cvt_pk_bf16_f32 v100, v112, v113\n\t" \
  "v_cvt_pk_bf16_f32 v102, v116, v117\n\t" \
  "v_cvt_pk_bf16_f32 v101, v114, v115\n\t" \
  "v_cvt_pk_bf16_f32 v103, v118, v119\n\t" \
  "v_add_u32 v20, 32768, v23\n\t" \
  "v_min_u32 v20, v20, v21\n\t" \
  "v_permlane16_swap_b32 v100, v102\n\t" \
  "v_permlane16_swap_b32 v101, v103\n\t" \
  "s_nop 0\n\t" \
  "global_store_dwordx4 v20, v[100:103], s[66:67] sc1\n\t" \
  "s_cmp_lt_u32 s54, 64\n\t" \
  "s_cbranch_scc0 NP2_%=\n\t" \
  "s_lshl_b32 s63, s56, 2\n\t" \
  "v_add_u32 v19, s63, v3\n\t" \
  "v_lshlrev_b32 v19, 15, v19\n\t" \
  "v_bfe_u32 v20, v2, 1, 1\n\t" \
  "v_lshl_add_u32 v19, v20, 7, v19\n\t" \
  "v_bfe_u32 v20, v2, 2, 1\n\t" \
  "v_lshl_add_u32 v19, v20, 6, v19\n\t" \
  "v_and_b32 v20, 9, v2\n\t" \
  "v_lshl_add_u32 v19, v20, 5, v19\n\t" \
  "s_lshl_b32 s63, s55, 9\n\t" \
  "s_add_u32 s63, s63, 8265728\n\t" \
  "s_add_u32 s64, s42, s63\n\t" \
  "s_addc_u32 s65, s43, 0\n\t" \
  "s_mov_b32 s63, 0x05040100\n\t" \
  "s_mov_b32 s54, 0x07060302\n\t" \
  "s_waitcnt vmcnt(3)\n\t" \
  "v_perm_b32 v24, v245, v244, s63\n\t" \
  "v_perm_b32 v25, v247, v246, s63\n\t" \
  "v_perm_b32 v26, v249, v248, s63\n\t" \
  "v_perm_b32 v27, v251, v250, s63\n\t" \
  "v_perm_b32 v28, v245, v244, s54\n\t" \
  "v_perm_b32 v29, v247, v246, s54\n\t" \
  "v_perm_b32 v30, v249, v248, s54\n\t" \
  "v_perm_b32 v31, v251, v250, s54\n\t" \
  "global_store_dwordx4 v19, v[24:27], s[64:65] sc1\n\t" \
  "global_store_dwordx4 v19, v[28:31], s[64:65] offset:16 sc1\n\t" \
  "NP2_%=:\n\t" \
  "s_waitcnt vmcnt(3)\n\t" \
  "s_branch TAIL1_%=\n\t" \
  "NOTG_%=:\n\t" \
  "s_cmp_eq_u32 s54, 1\n\t" \
  "s_cbranch_scc0 IDLE_%=\n\t" \
  "s_mul_i32 s63, s57, 16000\n\t" \
  "s_add_u32 s63, s63, s62\n\t" \
  "v_add_u32 v20, s63, v111\n\t" \
  "v_lshl_add_u32 v64, s53, 6, v1\n\t" \
  "v_cmp_gt_u32 vcc, 488, v64\n\t" \
  "global_load_dwordx4 v[24:27], v20, s[46:47]\n\t" \
  "v_add_u32 v21, 8192, v20\n\t" \
  "v_cndmask_b32 v21, v20, v21, vcc\n\t" \
  "global_load_dwordx4 v[28:31], v21, s[46:47]\n\t" \
  "global_load_dwordx4 v[32:35], v20, s[44:45]\n\t" \
  "global_load_dwordx4 v[36:39], v21, s[44:45]\n\t" \
  "v_mov_b32 v65, 1048576\n\t" \
  "v_cndmask_b32 v65, v65, 0, vcc\n\t" \
  "v_mov_b64 v[68:69], 0\n\t" \
  "v_mov_b64 v[70:71], 0\n\t" \
  "v_add_u32 v22, s62, v111\n\t" \
  "ds_write_b128 v22, v[68:71]\n\t" \
  "v_mul_u32_u24 v22, 49152, v1\n\t" \
  "v_add_u32 v22, 5120000, v22\n\t" \
  "global_load_dword v244, v20, s[46:47]\n\t" \
  "v_lshlrev_b32 v66, 16, v64\n\t" \
  "s_waitcnt vmcnt(3) lgkmcnt(0)\n\t" \
  "s_barrier\n\t" \
  "v_mov_b32 v67, 1\n\t" \
  "v_lshrrev_b32 v48, 8, v24\n\t" \
  "v_and_b32 v56, 0xff, v24\n\t" \
  "v_mul_u32_u24 v20, 37, v48\n\t" \
  "v_lshrrev_b32 v20, 8, v20\n\t" \
  "v_mul_u32_u24 v40, 24, v56\n\t" \
  "v_lshl_add_u32 v40, v20, 2, v40\n\t" \
  "v_add_u32 v24, v48, v20\n\t" \
  "ds_add_rtn_u32 v48, v40, v67\n\t" \
  "v_lshrrev_b32 v49, 8, v25\n\t" \
  "v_and_b32 v57, 0xff, v25\n\t" \
  "v_mul_u32_u24 v20, 37, v49\n\t" \
  "v_lshrrev_b32 v20, 8, v20\n\t" \
  "v_mul_u32_u24 v41, 24, v57\n\t" \
  "v_lshl_add_u32 v41, v20, 2, v41\n\t" \
  "v_add_u32 v25, v49, v20\n\t" \
  "ds_add_rtn_u32 v49, v41, v67\n\t" \
  "v_lshrrev_b32 v50, 8, v26\n\t" \
  "v_and_b32 v58, 0xff, v26\n\t" \
  "v_mul_u32_u24 v20, 37, v50\n\t" \
  "v_lshrrev_b32 v20, 8, v20\n\t" \
  "v_mul_u32_u24 v42, 24, v58\n\t" \
  "v_lshl_add_u32 v42, v20, 2, v42\n\t" \
  "v_add_u32 v26, v50, v20\n\t" \
  "ds_add_rtn_u32 v50, v42, v67\n\t" \
  "v_lshrrev_b32 v51, 8, v27\n\t" \
  "v_and_b32 v59, 0xff, v27\n\t" \
  "v_mul_u32_u24 v20, 37, v51\n\t" \
  "v_lshrrev_b32 v20, 8, v20\n\t" \
  "v_mul_u32_u24 v43, 24, v59\n\t" \
  "v_lshl_add_u32 v43, v20, 2, v43\n\t" \
  "v_add_u32 v27, v51, v20\n\t" \
  "ds_add_rtn_u32 v51, v43, v67\n\t" \
  "v_lshrrev_b32 v52, 8, v28\n\t" \
  "v_and_b32 v60, 0xff, v28\n\t" \
  "v_mul_u32_u24 v20, 37, v52\n\t" \
  "v_lshrrev_b32 v20, 8, v20\n\t" \
  "v_mad_u32_u24 v44, v60, 24, v65\n\t" \
  "v_lshl_add_u32 v44, v20, 2, v44\n\t" \
  "v_add_u32 v28, v52, v20\n\t" \
  "ds_add_rtn_u32 v52, v44, v67\n\t" \
  "v_lshrrev_b32 v53, 8, v29\n\t" \
  "v_and_b32 v61, 0xff, v29\n\t" \
  "v_mul_u32_u24 v20, 37, v53\n\t" \
  "v_lshrrev_b32 v20, 8, v20\n\t" \
  "v_mad_u32_u24 v45, v61, 24, v65\n\t" \
  "v_lshl_add_u32 v45, v20, 2, v45\n\t" \
  "v_add_u32 v29, v53, v20\n\t" \
  "ds_add_rtn_u32 v53, v45, v67\n\t" \
  "v_lshrrev_b32 v54, 8, v30\n\t" \
  "v_and_b32 v62, 0xff, v30\n\t" \
  "v_mul_u32_u24 v20, 37, v54\n\t" \
  "v_lshrrev_b32 v20, 8, v20\n\t" \
  "v_mad_u32_u24 v46, v62, 24, v65\n\t" \
  "v_lshl_add_u32 v46, v20, 2, v46\n\t" \
  "v_add_u32 v30, v54, v20\n\t" \
  "ds_add_rtn_u32 v54, v46, v67\n\t" \
  "v_lshrrev_b32 v55, 8, v31\n\t" \
  "v_and_b32 v63, 0xff, v31\n\t" \
  "v_mul_u32_u24 v20, 37, v55\n\t" \
  "v_lshrrev_b32 v20, 8, v20\n\t" \
  "v_mad_u32_u24 v47, v63, 24, v65\n\t" \
  "v_lshl_add_u32 v47, v20, 2, v47\n\t" \
  "v_add_u32 v31, v55, v20\n\t" \
  "ds_add_rtn_u32 v55, v47, v67\n\t" \
  "s_waitcnt vmcnt(1)\n\t" \
  "v_lshl_add_u32 v17, v15, 8, s52\n\t" \
  "v_lshl_add_u32 v18, v17, 9, v16\n\t" \
  "s_add_u32 m0, s62, 131072\n\t" \
  "s_nop 0\n\t" \
  "global_load_lds_dwordx4 v18, s[36:37]\n\t" \
  "v_add_u32 v18, 2097152, v18\n\t" \
  "s_add_u32 m0, s62, 139264\n\t" \
  "s_nop 0\n\t" \
  "global_load_lds_dwordx4 v18, s[36:37]\n\t" \
  "s_add_u32 s63, s62, 262144\n\t" \
  "v_add_u32 v19, s63, v111\n\t" \
  "s_add_u32 m0, s62, 151552\n\t" \
  "s_nop 0\n\t" \
  "global_load_lds_dwordx4 v19, s[38:39]\n\t" \
  "v_add_u32 v17, 8192, v17\n\t" \
  "v_min_u32 v17, 9999, v17\n\t" \
  "v_lshl_add_u32 v17, v17, 9, v16\n\t" \
  "s_cmp_lt_u32 s53, 4\n\t" \
  "s_cbranch_scc0 XAs_%=\n\t" \
  "s_add_u32 m0, s62, 147456\n\t" \
  "s_nop 0\n\t" \
  "global_load_lds_dwordx4 v17, s[36:37]\n\t" \
  "XAs_%=:\n\t" \
  "s_cmp_eq_u32 s53, 4\n\t" \
  "s_cbranch_scc0 XBs_%=\n\t" \
  "s_mov_b32 exec_hi, 0\n\t" \
  "v_add_u32 v19, 270336, v111\n\t" \
  "s_mov_b32 m0, 159744\n\t" \
  "s_nop 0\n\t" \
  "global_load_lds_dwordx4 v19, s[38:39]\n\t" \
  "s_mov_b32 m0, 160256\n\t" \
  "s_nop 0\n\t" \
  "global_load_lds_dwordx4 v111, s[50:51]\n\t" \
  "s_mov_b32 exec_hi, -1\n\t" \
  "XBs_%=:\n\t" \
  "v_lshl_or_b32 v32, v24, 26, v32\n\t" \
  "s_mov_b32 s63, 0\n\t" \
  "v_add3_u32 v32, v32, v66, s63\n\t" \
  "s_movk_i32 s63, 208\n\t" \
  "v_mul_u32_u24 v56, s63, v56\n\t" \
  "v_lshl_or_b32 v33, v25, 26, v33\n\t" \
  "s_mov_b32 s63, 16384\n\t" \
  "v_add3_u32 v33, v33, v66, s63\n\t" \
  "s_movk_i32 s63, 208\n\t" \
  "v_mul_u32_u24 v57, s63, v57\n\t" \
  "v_lshl_or_b32 v34, v26, 26, v34\n\t" \
  "s_mov_b32 s63, 32768\n\t" \
  "v_add3_u32 v34, v34, v66, s63\n\t" \
  "s_movk_i32 s63, 208\n\t" \
  "v_mul_u32_u24 v58, s63, v58\n\t" \
  "v_lshl_or_b32 v35, v27, 26, v35\n\t" \
  "s_mov_b32 s63, 49152\n\t" \
  "v_add3_u32 v35, v35, v66, s63\n\t" \
  "s_movk_i32 s63, 208\n\t" \
  "v_mul_u32_u24 v59, s63, v59\n\t" \
  "v_lshl_or_b32 v36, v28, 26, v36\n\t" \
  "s_mov_b32 s63, 33554432\n\t" \
  "v_add3_u32 v36, v36, v66, s63\n\t" \
  "s_movk_i32 s63, 208\n\t" \
  "v_mad_u32_u24 v60, v60, s63, v65\n\t" \
  "v_lshl_or_b32 v37, v29, 26, v37\n\t" \
  "s_mov_b32 s63, 33570816\n\t" \
  "v_add3_u32 v37, v37, v66, s63\n\t" \
  "s_movk_i32 s63, 208\n\t" \
  "v_mad_u32_u24 v61, v61, s63, v65\n\t" \
  "v_lshl_or_b32 v38, v30, 26, v38\n\t" \
  "s_mov_b32 s63, 33587200\n\t" \
  "v_add3_u32 v38, v38, v66, s63\n\t" \
  "s_movk_i32 s63, 208\n\t" \
  "v_mad_u32_u24 v62, v62, s63, v65\n\t" \
  "v_lshl_or_b32 v39, v31, 26, v39\n\t" \
  "s_mov_b32 s63, 33603584\n\t" \
  "v_add3_u32 v39, v39, v66, s63\n\t" \
  "s_movk_i32 s63, 208\n\t" \
  "v_mad_u32_u24 v63, v63, s63, v65\n\t" \
  "s_waitcnt lgkmcnt(0)\n\t" \
  "s_barrier\n\t" \
  "s_cmp_lt_u32 s53, 4\n\t" \
  "s_cbranch_scc0 NOPF_%=\n\t" \
  "v_mul_u32_u24 v20, 24, v64\n\t" \
  "ds_read_b64 v[68:69], v20\n\t" \
  "ds_read_b64 v[70:71], v20 offset:8\n\t" \
  "ds_read_b64 v[72:73], v20 offset:16\n\t" \
  "s_lshl_b32 s63, s57, 5\n\t" \
  "s_add_u32 s63, s63, 7741440\n\t" \
  "s_add_u32 s64, s42, s63\n\t" \
  "s_addc_u32 s65, s43, 0\n\t" \
  "v_lshlrev_b32 v21, 11, v64\n\t" \
  "v_lshlrev_b32 v22, 2, v64\n\t" \
  "v_mov_b32 v74, 0\n\t" \
  "v_mov_b32 v87, 0\n\t" \
  "s_waitcnt lgkmcnt(0)\n\t" \
  "v_mov_b32 v75, v68\n\t" \
  "v_add_u32 v76, v75, v69\n\t" \
  "v_add_u32 v77, v76, v70\n\t" \
  "v_add_u32 v78, v77, v71\n\t" \
  "v_add_u32 v79, v78, v72\n\t" \
  "v_add_u32 v83, v79, v73\n\t" \
  "ds_write_b64 v20, v[74:75]\n\t" \
  "ds_write_b64 v20, v[76:77] offset:8\n\t" \
  "ds_write_b64 v20, v[78:79] offset:16\n\t" \
  "ds_write_b32 v22, v83 offset:6144\n\t" \
  "v_lshl_or_b32 v80, v69, 16, v68\n\t" \
  "v_lshl_or_b32 v81, v71, 16, v70\n\t" \
  "v_lshl_or_b32 v82, v73, 16, v72\n\t" \
  "v_lshl_or_b32 v84, v76, 16, v75\n\t" \
  "v_lshl_or_b32 v85, v78, 16, v77\n\t" \
  "v_lshl_or_b32 v86, v83, 16, v79\n\t" \
  "global_store_dwordx4 v21, v[80:83], s[64:65] sc1\n\t" \
  "global_store_dwordx4 v21, v[84:87], s[64:65] offset:16 sc1\n\t" \
  "s_waitcnt lgkmcnt(0)\n\t" \
  "NOPF_%=:\n\t" \
  "s_barrier\n\t" \
  "ds_read_b32 v40, v40\n\t" \
  "ds_read_b32 v41, v41\n\t" \
  "ds_read_b32 v42, v42\n\t" \
  "ds_read_b32 v43, v43\n\t" \
  "ds_read_b32 v44, v44\n\t" \
  "ds_read_b32 v45, v45\n\t" \
  "ds_read_b32 v46, v46\n\t" \
  "ds_read_b32 v47, v47\n\t" \
  "s_waitcnt lgkmcnt(7)\n\t" \
  "v_add_u32 v48, v48, v40\n\t" \
  "v_min_u32 v48, 48, v48\n\t" \
  "v_lshl_add_u32 v48, v48, 2, v56\n\t" \
  "s_waitcnt lgkmcnt(6)\n\t" \
  "v_add_u32 v49, v49, v41\n\t" \
  "v_min_u32 v49, 48, v49\n\t" \
  "v_lshl_add_u32 v49, v49, 2, v57\n\t" \
  "s_waitcnt lgkmcnt(5)\n\t" \
  "v_add_u32 v50, v50, v42\n\t" \
  "v_min_u32 v50, 48, v50\n\t" \
  "v_lshl_add_u32 v50, v50, 2, v58\n\t" \
  "s_waitcnt lgkmcnt(4)\n\t" \
  "v_add_u32 v51, v51, v43\n\t" \
  "v_min_u32 v51, 48, v51\n\t" \
  "v_lshl_add_u32 v51, v51, 2, v59\n\t" \
  "s_waitcnt lgkmcnt(3)\n\t" \
  "v_add_u32 v52, v52, v44\n\t" \
  "v_min_u32 v52, 48, v52\n\t" \
  "v_lshl_add_u32 v52, v52, 2, v60\n\t" \
  "s_waitcnt lgkmcnt(2)\n\t" \
  "v_add_u32 v53, v53, v45\n\t" \
  "v_min_u32 v53, 48, v53\n\t" \
  "v_lshl_add_u32 v53, v53, 2, v61\n\t" \
  "s_waitcnt lgkmcnt(1)\n\t" \
  "v_add_u32 v54, v54, v46\n\t" \
  "v_min_u32 v54, 48, v54\n\t" \
  "v_lshl_add_u32 v54, v54, 2, v62\n\t" \
  "s_waitcnt lgkmcnt(0)\n\t" \
  "v_add_u32 v55, v55, v47\n\t" \
  "v_min_u32 v55, 48, v55\n\t" \
  "v_lshl_add_u32 v55, v55, 2, v63\n\t" \
  "ds_write_b32 v48, v32 offset:7168\n\t" \
  "ds_write_b32 v49, v33 offset:7168\n\t" \
  "ds_write_b32 v50, v34 offset:7168\n\t" \
  "ds_write_b32 v51, v35 offset:7168\n\t" \
  "ds_write_b32 v52, v36 offset:7168\n\t" \
  "ds_write_b32 v53, v37 offset:7168\n\t" \
  "ds_write_b32 v54, v38 offset:7168\n\t" \
  "ds_write_b32 v55, v39 offset:7168\n\t" \
  "v_lshrrev_b32 v20, 3, v64\n\t" \
  "v_and_b32 v21, 7, v64\n\t" \
  "v_lshlrev_b32 v21, 4, v21\n\t" \
  "s_movk_i32 s63, 208\n\t" \
  "v_mad_u32_u24 v22, v20, s63, v21\n\t" \
  "s_movk_i32 s66, 10240\n\t" \
  "v_mad_u32_u24 v23, v20, s66, v21\n\t" \
  "v_lshrrev_b32 v20, 2, v64\n\t" \
  "v_and_b32 v21, 3, v64\n\t" \
  "v_lshlrev_b32 v21, 4, v21\n\t" \
  "v_mad_u32_u24 v66, v20, s63, v21\n\t" \
  "v_mad_u32_u24 v67, v20, s66, v21\n\t" \
  "v_lshlrev_b32 v20, 2, v20\n\t" \
  "s_lshl_b32 s63, s57, 8\n\t" \
  "s_add_u32 s63, s63, 5120000\n\t" \
  "s_add_u32 s64, s42, s63\n\t" \
  "s_addc_u32 s65, s43, 0\n\t" \
  "s_waitcnt lgkmcnt(0)\n\t" \
  "s_barrier\n\t" \
  "ds_read_b128 v[24:27], v22 offset:7168\n\t" \
  "ds_read_b128 v[28:31], v22 offset:20480\n\t" \
  "ds_read_b128 v[32:35], v22 offset:33792\n\t" \
  "ds_read_b128 v[36:39], v22 offset:47104\n\t" \
  "ds_read_b32 v68, v20 offset:6144\n\t" \
  "ds_read_b32 v69, v20 offset:6656\n\t" \
  "ds_read_b128 v[40:43], v66 offset:7296\n\t" \
  "ds_read_b128 v[44:47], v66 offset:33920\n\t" \
  "s_waitcnt lgkmcnt(7)\n\t" \
  "global_store_dwordx4 v23, v[24:27], s[64:65] sc1\n\t" \
  "v_add_u32 v23, 655360, v23\n\t" \
  "s_waitcnt lgkmcnt(6)\n\t" \
  "global_store_dwordx4 v23, v[28:31], s[64:65] sc1\n\t" \
  "v_add_u32 v23, 655360, v23\n\t" \
  "s_waitcnt lgkmcnt(5)\n\t" \
  "global_store_dwordx4 v23, v[32:35], s[64:65] sc1\n\t" \
  "v_add_u32 v23, 655360, v23\n\t" \
  "s_waitcnt lgkmcnt(4)\n\t" \
  "global_store_dwordx4 v23, v[36:39], s[64:65] sc1\n\t" \
  "s_waitcnt lgkmcnt(0)\n\t" \
  "v_max_u32 v70, v68, v69\n\t" \
  "v_cmp_lt_u32 vcc, 32, v70\n\t" \
  "s_cbranch_vccz NOCX_%=\n\t" \
  "v_cmp_lt_u32 vcc, 32, v68\n\t" \
  "s_mov_b64 exec, vcc\n\t" \
  "global_store_dwordx4 v67, v[40:43], s[64:65] offset:128 sc1\n\t" \
  "s_mov_b64 exec, -1\n\t" \
  "v_add_u32 v67, 1310720, v67\n\t" \
  "v_cmp_lt_u32 vcc, 32, v69\n\t" \
  "s_mov_b64 exec, vcc\n\t" \
  "global_store_dwordx4 v67, v[44:47], s[64:65] offset:128 sc1\n\t" \
  "s_mov_b64 exec, -1\n\t" \
  "NOCX_%=:\n\t" \
  "v_lshrrev_b32 v20, 2, v2\n\t" \
  "v_lshl_add_u32 v21, v3, 3, v20\n\t" \
  "v_lshlrev_b32 v21, 8, v21\n\t" \
  "v_and_b32 v22, 1, v2\n\t" \
  "v_lshl_add_u32 v21, v22, 3, v21\n\t" \
  "v_bfe_u32 v22, v2, 1, 1\n\t" \
  "s_and_b32 s63, s53, 3\n\t" \
  "s_lshl_b32 s63, s63, 2\n\t" \
  "v_or_b32 v22, s63, v22\n\t" \
  "v_lshlrev_b32 v20, 2, v20\n\t" \
  "v_xor_b32 v22, v22, v20\n\t" \
  "v_and_b32 v20, 1, v3\n\t" \
  "v_lshlrev_b32 v20, 1, v20\n\t" \
  "v_xor_b32 v22, v22, v20\n\t" \
  "v_lshl_add_u32 v7, v22, 4, v21\n\t" \
  "v_xor_b32 v8, 16, v7\n\t" \
  "v_add_u32 v8, 1024, v8\n\t" \
  "s_waitcnt vmcnt(4)\n\t" \
  "s_branch TAIL1_%=\n\t" \
  "IDLE_%=:\n\t" \
  "v_lshl_add_u32 v17, v15, 8, s52\n\t" \
  "v_lshl_add_u32 v18, v17, 9, v16\n\t" \
  "s_add_u32 m0, s62, 131072\n\t" \
  "s_nop 0\n\t" \
  "global_load_lds_dwordx4 v18, s[36:37]\n\t" \
  "v_add_u32 v18, 2097152, v18\n\t" \
  "s_add_u32 m0, s62, 139264\n\t" \
  "s_nop 0\n\t" \
  "global_load_lds_dwordx4 v18, s[36:37]\n\t" \
  "s_add_u32 s63, s62, 262144\n\t" \
  "v_add_u32 v19, s63, v111\n\t" \
  "s_add_u32 m0, s62, 151552\n\t" \
  "s_nop 0\n\t" \
  "global_load_lds_dwordx4 v19, s[38:39]\n\t" \
  "v_add_u32 v17, 8192, v17\n\t" \
  "v_min_u32 v17, 9999, v17\n\t" \
  "v_lshl_add_u32 v17, v17, 9, v16\n\t" \
  "s_cmp_lt_u32 s53, 4\n\t" \
  "s_cbranch_scc0 XAi_%=\n\t" \
  "s_add_u32 m0, s62, 147456\n\t" \
  "s_nop 0\n\t" \
  "global_load_lds_dwordx4 v17, s[36:37]\n\t" \
  "XAi_%=:\n\t" \
  "s_cmp_eq_u32 s53, 4\n\t" \
  "s_cbranch_scc0 XBi_%=\n\t" \
  "s_mov_b32 exec_hi, 0\n\t" \
  "v_add_u32 v19, 270336, v111\n\t" \
  "s_mov_b32 m0, 159744\n\t" \
  "s_nop 0\n\t" \
  "global_load_lds_dwordx4 v19, s[38:39]\n\t" \
  "s_mov_b32 m0, 160256\n\t" \
  "s_nop 0\n\t" \
  "global_load_lds_dwordx4 v111, s[50:51]\n\t" \
  "s_mov_b32 exec_hi, -1\n\t" \
  "XBi_%=:\n\t" \
  "v_lshrrev_b32 v20, 2, v2\n\t" \
  "v_lshl_add_u32 v21, v3, 3, v20\n\t" \
  "v_lshlrev_b32 v21, 8, v21\n\t" \
  "v_and_b32 v22, 1, v2\n\t" \
  "v_lshl_add_u32 v21, v22, 3, v21\n\t" \
  "v_bfe_u32 v22, v2, 1, 1\n\t" \
  "s_and_b32 s63, s53, 3\n\t" \
  "s_lshl_b32 s63, s63, 2\n\t" \
  "v_or_b32 v22, s63, v22\n\t" \
  "v_lshlrev_b32 v20, 2, v20\n\t" \
  "v_xor_b32 v22, v22, v20\n\t" \
  "v_and_b32 v20, 1, v3\n\t" \
  "v_lshlrev_b32 v20, 1, v20\n\t" \
  "v_xor_b32 v22, v22, v20\n\t" \
  "v_lshl_add_u32 v7, v22, 4, v21\n\t" \
  "v_xor_b32 v8, 16, v7\n\t" \
  "v_add_u32 v8, 1024, v8\n\t" \
  "s_waitcnt vmcnt(0)\n\t" \
  "TAIL1_%=:\n\t" \
  "s_and_b32 s63, s52, 7\n\t" \
  "s_lshl_b32 s63, s63, 7\n\t" \
  "s_add_u32 s63, s63, 8528896\n\t" \
  "s_add_u32 s66, s42, s63\n\t" \
  "s_addc_u32 s67, s43, 0\n\t" \
  "v_mov_b32 v110, 0\n\t" \
  "s_cmp_eq_u32 s53, 0\n\t" \
  "s_cbranch_scc0 NG_%=\n\t" \
  "global_load_dwordx2 v[126:127], v110, s[66:67] sc1\n\t" \
  "NG_%=:\n\t" \
  "s_sub_u32 s63, s53, 1\n\t" \
  "s_cmp_lt_u32 s63, 2\n\t" \
  "s_cbranch_scc0 NM_%=\n\t" \
  "v_lshl_add_u32 v116, s63, 6, v1\n\t" \
  "v_mul_u32_u24 v117, 171, v116\n\t" \
  "v_lshrrev_b32 v117, 9, v117\n\t" \
  "v_mad_i32_i24 v116, v117, -3, v116\n\t" \
  "v_lshl_add_u32 v118, v117, 8, s52\n\t" \
  "v_cmp_gt_u32 vcc, 10000, v118\n\t" \
  "v_min_u32 v118, 9999, v118\n\t" \
  "v_mad_u32_u24 v118, v118, 3, v116\n\t" \
  "v_lshlrev_b32 v118, 1, v118\n\t" \
  "global_load_ushort v11, v118, s[48:49]\n\t" \
  "v_mul_u32_u24 v118, 37, v117\n\t" \
  "v_lshrrev_b32 v118, 8, v118\n\t" \
  "v_add_lshl_u32 v117, v117, v118, 2\n\t" \
  "v_add_lshl_u32 v117, v117, v116, 2\n\t" \
  "v_mov_b32 v116, 1048576\n\t" \
  "v_add_u32 v117, 160768, v117\n\t" \
  "v_cndmask_b32 v125, v116, v117, vcc\n\t" \
  "NM_%=:\n\t" \
  "s_waitcnt lgkmcnt(0)\n\t" \
  "s_barrier\n\t" \
  "s_add_u32 m0, s62, 65536\n\t" \
  "s_add_u32 s64, s38, 0\n\t" \
  "s_addc_u32 s65, s39, 0\n\t" \
  "global_load_lds_dwordx4 v6, s[64:65]\n\t" \
  "s_add_u32 m0, s62, 73728\n\t" \
  "s_add_u32 s64, s38, 16384\n\t" \
  "s_addc_u32 s65, s39, 0\n\t" \
  "global_load_lds_dwordx4 v6, s[64:65]\n\t" \
  "s_add_u32 m0, s62, 81920\n\t" \
  "s_add_u32 s64, s38, 32768\n\t" \
  "s_addc_u32 s65, s39, 0\n\t" \
  "global_load_lds_dwordx4 v6, s[64:65]\n\t" \
  "s_add_u32 m0, s62, 90112\n\t" \
  "s_add_u32 s64, s38, 49152\n\t" \
  "s_addc_u32 s65, s39, 0\n\t" \
  "global_load_lds_dwordx4 v6, s[64:65]\n\t" \
  "s_add_u32 m0, s62, 0\n\t" \
  "s_add_u32 s64, s38, 256\n\t" \
  "s_addc_u32 s65, s39, 0\n\t" \
  "global_load_lds_dwordx4 v6, s[64:65]\n\t" \
  "s_add_u32 m0, s62, 8192\n\t" \
  "s_add_u32 s64, s38, 16640\n\t" \
  "s_addc_u32 s65, s39, 0\n\t" \
  "global_load_lds_dwordx4 v6, s[64:65]\n\t" \
  "s_add_u32 m0, s62, 16384\n\t" \
  "s_add_u32 s64, s38, 33024\n\t" \
  "s_addc_u32 s65, s39, 0\n\t" \
  "global_load_lds_dwordx4 v6, s[64:65]\n\t" \
  "s_add_u32 m0, s62, 24576\n\t" \
  "s_add_u32 s64, s38, 49408\n\t" \
  "s_addc_u32 s65, s39, 0\n\t" \
  "global_load_lds_dwordx4 v6, s[64:65]\n\t" \
  "v_lshl_add_u32 v112, v4, 3, 1\n\t" \
  "v_lshrrev_b32 v113, 2, v2\n\t" \
  "v_add_lshl_u32 v112, v112, v113, 9\n\t" \
  "v_and_b32 v113, 1, v3\n\t" \
  "v_lshl_add_u32 v112, v113, 5, v112\n\t" \
  "v_and_b32 v113, 3, v2\n\t" \
  "v_lshl_add_u32 v112, v113, 3, v112\n\t" \
  "s_lshl_b32 s63, s53, 6\n\t" \
  "s_add_u32 s63, s63, 151552\n\t" \
  "v_add_u32 v112, s63, v112\n\t" \
  "v_lshl_add_u32 v113, v5, 1, s63\n\t" \
  "v_lshl_add_u32 v114, v4, 3, s63\n\t" \
  "ds_read_b64_tr_b16 v[96:97], v112\n\t" \
  "ds_read_b64_tr_b16 v[98:99], v112 offset:2048\n\t" \
  "ds_read_u16 v124, v113\n\t" \
  "v_add_u32 v114, 8704, v114\n\t" \
  "ds_read2_b64 v[100:103], v114 offset1:2\n\t" \
  "ds_read2_b64 v[104:107], v114 offset0:4 offset1:6\n\t" \
  "s_mov_b32 s68, 0xbfb8aa3b\n\t" \
  "s_mov_b32 s69, 0xbfb8aa3b\n\t" \
  "s_mov_b32 s70, 0xbf317218\n\t" \
  "s_mov_b32 s71, 0xbf317218\n\t" \
  "s_cmp_lt_u32 s53, 4\n\t" \
  "s_cselect_b32 s63, 65536, 0\n\t" \
  "v_add_u32 v7, s63, v7\n\t" \
  "v_add_u32 v8, s63, v8\n\t" \
  "v_xor_b32 v9, 32, v7\n\t" \
  "v_xor_b32 v10, 32, v8\n\t" \
  "v_bfe_u32 v112, v2, 2, 1\n\t" \
  "v_mul_u32_u24 v112, 7, v112\n\t" \
  "v_and_b32 v113, 3, v2\n\t" \
  "v_add_u32 v112, v112, v113\n\t" \
  "v_lshrrev_b32 v113, 3, v2\n\t" \
  "v_lshl_add_u32 v112, v113, 2, v112\n\t" \
  "v_and_b32 v113, 15, v112\n\t" \
  "v_xor_b32 v113, v113, v3\n\t" \
  "v_lshlrev_b32 v113, 4, v113\n\t" \
  "v_lshl_add_u32 v13, v112, 9, v113\n\t" \
  "v_add_u32 v13, 131072, v13\n\t" \
  "v_add_u32 v112, 14, v112\n\t" \
  "v_and_b32 v113, 15, v112\n\t" \
  "v_xor_b32 v113, v113, v3\n\t" \
  "v_lshlrev_b32 v113, 4, v113\n\t" \
  "v_lshl_add_u32 v14, v112, 9, v113\n\t" \
  "v_add_u32 v14, 131072, v14\n\t" \
  "v_add_u32 v112, 14, v112\n\t" \
  "v_min_u32 v112, 39, v112\n\t" \
  "v_and_b32 v113, 15, v112\n\t" \
  "v_xor_b32 v113, v113, v3\n\t" \
  "v_lshlrev_b32 v113, 4, v113\n\t" \
  "v_lshl_add_u32 v15, v112, 9, v113\n\t" \
  "v_add_u32 v15, 131072, v15\n\t" \
  "s_waitcnt vmcnt(8)\n\t" \
  "s_barrier\n\t" \
  "s_cmp_eq_u32 s53, 0\n\t" \
  "s_cbranch_scc0 NA_%=\n\t" \
  "v_readfirstlane_b32 s60, v126\n\t" \
  "v_readfirstlane_b32 s63, v127\n\t" \
  "s_cmp_eq_u32 s59, 0\n\t" \
  "s_cselect_b32 s58, s63, s58\n\t" \
  "s_lshl_b32 s63, s52, 2\n\t" \
  "s_add_u32 s63, s63, 8527872\n\t" \
  "v_mov_b32 v126, s63\n\t" \
  "v_mov_b32 v127, s58\n\t" \
  "s_mov_b64 exec, 1\n\t" \
  "global_store_dword v126, v127, s[42:43] sc1\n\t" \
  "s_mov_b64 exec, -1\n\t" \
  "s_waitcnt vmcnt(1)\n\t" \
  "s_branch NB_%=\n\t" \
  "NA_%=:\n\t" \
  "s_waitcnt vmcnt(0)\n\t" \
  "NB_%=:\n\t" \
  "s_barrier\n\t" \
  "ds_read_b64_tr_b16 v[144:145], v7 offset:0\n\t" \
  "ds_read_b64_tr_b16 v[146:147], v8 offset:0\n\t" \
  "ds_read_b64_tr_b16 v[148:149], v9 offset:0\n\t" \
  "ds_read_b64_tr_b16 v[150:151], v10 offset:0\n\t" \
  "ds_read_b128 v[40:43], v13\n\t" \
  "ds_read_b128 v[44:47], v14\n\t" \
  "ds_read_b128 v[48:51], v15\n\t" \
  "s_add_u32 m0, s62, 98304\n\t" \
  "s_add_u32 s64, s38, 65536\n\t" \
  "s_addc_u32 s65, s39, 0\n\t" \
  "global_load_lds_dwordx4 v6, s[64:65]\n\t" \
  "ds_read_b64_tr_b16 v[152:153], v7 offset:8192\n\t" \
  "ds_read_b64_tr_b16 v[154:155], v8 offset:8192\n\t" \
  "ds_read_b64_tr_b16 v[156:157], v9 offset:8192\n\t" \
  "ds_read_b64_tr_b16 v[158:159], v10 offset:8192\n\t" \
  "v_xor_b32 v120, 64, v13\n\t" \
  "ds_read_b128 v[52:55], v120\n\t" \
  "v_xor_b32 v121, 64, v14\n\t" \
  "ds_read_b128 v[56:59], v121\n\t" \
  "v_xor_b32 v122, 64, v15\n\t" \
  "ds_read_b128 v[60:63], v122\n\t" \
  "s_add_u32 m0, s62, 32768\n\t" \
  "s_add_u32 s64, s38, 65792\n\t" \
  "s_addc_u32 s65, s39, 0\n\t" \
  "global_load_lds_dwordx4 v6, s[64:65]\n\t" \
  "ds_read_b64_tr_b16 v[160:161], v7 offset:16384\n\t" \
  "ds_read_b64_tr_b16 v[162:163], v8 offset:16384\n\t" \
  "ds_read_b64_tr_b16 v[164:165], v9 offset:16384\n\t" \
  "ds_read_b64_tr_b16 v[166:167], v10 offset:16384\n\t" \
  "v_xor_b32 v120, 128, v13\n\t" \
  "ds_read_b128 v[64:67], v120\n\t" \
  "v_xor_b32 v121, 128, v14\n\t" \
  "ds_read_b128 v[68:71], v121\n\t" \
  "v_xor_b32 v122, 128, v15\n\t" \
  "ds_read_b128 v[72:75], v122\n\t" \
  "s_add_u32 m0, s62, 106496\n\t" \
  "s_add_u32 s64, s38, 81920\n\t" \
  "s_addc_u32 s65, s39, 0\n\t" \
  "global_load_lds_dwordx4 v6, s[64:65]\n\t" \
  "ds_read_b64_tr_b16 v[168:169], v7 offset:24576\n\t" \
  "ds_read_b64_tr_b16 v[170:171], v8 offset:24576\n\t" \
  "ds_read_b64_tr_b16 v[172:173], v9 offset:24576\n\t" \
  "ds_read_b64_tr_b16 v[174:175], v10 offset:24576\n\t" \
  "v_xor_b32 v120, 192, v13\n\t" \
  "ds_read_b128 v[76:79], v120\n\t" \
  "v_xor_b32 v121, 192, v14\n\t" \
  "ds_read_b128 v[80:83], v121\n\t" \
  "v_xor_b32 v122, 192, v15\n\t" \
  "ds_read_b128 v[84:87], v122\n\t" \
  "s_add_u32 m0, s62, 40960\n\t" \
  "s_add_u32 s64, s38, 82176\n\t" \
  "s_addc_u32 s65, s39, 0\n\t" \
  "global_load_lds_dwordx4 v6, s[64:65]\n\t" \
  "s_sub_u32 s63, s53, 1\n\t" \
  "s_cmp_lt_u32 s63, 2\n\t" \
  "s_cbranch_scc0 NM2_%=\n\t" \
  "v_lshlrev_b32 v11, 16, v11\n\t" \
  "ds_write_b32 v125, v11\n\t" \
  "NM2_%=:\n\t" \
  "s_waitcnt lgkmcnt(15)\n\t" \
  "v_mfma_f32_16x16x32_bf16 v[16:19], v[40:43], v[144:147], 0\n\t" \
  "v_mfma_f32_16x16x32_bf16 v[20:23], v[40:43], v[148:151], 0\n\t" \
  "s_add_u32 m0, s62, 114688\n\t" \
  "s_add_u32 s64, s38, 98304\n\t" \
  "s_addc_u32 s65, s39, 0\n\t" \
  "global_load_lds_dwordx4 v6, s[64:65]\n\t" \
  "s_waitcnt lgkmcnt(15)\n\t" \
  "v_mfma_f32_16x16x32_bf16 v[24:27], v[44:47], v[144:147], 0\n\t" \
  "v_mfma_f32_16x16x32_bf16 v[28:31], v[44:47], v[148:151], 0\n\t" \
  "s_waitcnt lgkmcnt(15)\n\t" \
  "v_mfma_f32_16x16x32_bf16 v[32:35], v[48:51], v[144:147], 0\n\t" \
  "s_add_u32 m0, s62, 49152\n\t" \
  "s_add_u32 s64, s38, 98560\n\t" \
  "s_addc_u32 s65, s39, 0\n\t" \
  "global_load_lds_dwordx4 v6, s[64:65]\n\t" \
  "v_mfma_f32_16x16x32_bf16 v[36:39], v[48:51], v[148:151], 0\n\t" \
  "s_waitcnt lgkmcnt(15)\n\t" \
  "v_mfma_f32_16x16x32_bf16 v[16:19], v[52:55], v[152:155], v[16:19]\n\t" \
  "v_mfma_f32_16x16x32_bf16 v[20:23], v[52:55], v[156:159], v[20:23]\n\t" \
  "s_add_u32 m0, s62, 122880\n\t" \
  "s_add_u32 s64, s38, 114688\n\t" \
  "s_addc_u32 s65, s39, 0\n\t" \
  "global_load_lds_dwordx4 v6, s[64:65]\n\t" \
  "s_waitcnt lgkmcnt(15)\n\t" \
  "v_mfma_f32_16x16x32_bf16 v[24:27], v[56:59], v[152:155], v[24:27]\n\t" \
  "v_mfma_f32_16x16x32_bf16 v[28:31], v[56:59], v[156:159], v[28:31]\n\t" \
  "s_waitcnt lgkmcnt(14)\n\t" \
  "v_mfma_f32_16x16x32_bf16 v[32:35], v[60:63], v[152:155], v[32:35]\n\t" \
  "s_add_u32 m0, s62, 57344\n\t" \
  "s_add_u32 s64, s38, 114944\n\t" \
  "s_addc_u32 s65, s39, 0\n\t" \
  "global_load_lds_dwordx4 v6, s[64:65]\n\t" \
  "v_mfma_f32_16x16x32_bf16 v[36:39], v[60:63], v[156:159], v[36:39]\n\t" \
  "s_waitcnt lgkmcnt(9)\n\t" \
  "v_mfma_f32_16x16x32_bf16 v[16:19], v[64:67], v[160:163], v[16:19]\n\t" \
  "v_mfma_f32_16x16x32_bf16 v[20:23], v[64:67], v[164:167], v[20:23]\n\t" \
  "s_waitcnt lgkmcnt(8)\n\t" \
  "v_mfma_f32_16x16x32_bf16 v[24:27], v[68:71], v[160:163], v[24:27]\n\t" \
  "v_mfma_f32_16x16x32_bf16 v[28:31], v[68:71], v[164:167], v[28:31]\n\t" \
  "s_waitcnt lgkmcnt(7)\n\t" \
  "v_mfma_f32_16x16x32_bf16 v[32:35], v[72:75], v[160:163], v[32:35]\n\t" \
  "v_mfma_f32_16x16x32_bf16 v[36:39], v[72:75], v[164:167], v[36:39]\n\t" \
  "s_waitcnt lgkmcnt(2)\n\t" \
  "v_mfma_f32_16x16x32_bf16 v[16:19], v[76:79], v[168:171], v[16:19]\n\t" \
  "v_mfma_f32_16x16x32_bf16 v[20:23], v[76:79], v[172:175], v[20:23]\n\t" \
  "s_waitcnt lgkmcnt(1)\n\t" \
  "v_mfma_f32_16x16x32_bf16 v[24:27], v[80:83], v[168:171], v[24:27]\n\t" \
  "v_mfma_f32_16x16x32_bf16 v[28:31], v[80:83], v[172:175], v[28:31]\n\t" \
  "s_waitcnt lgkmcnt(0)\n\t" \
  "v_mfma_f32_16x16x32_bf16 v[32:35], v[84:87], v[168:171], v[32:35]\n\t" \
  "v_mfma_f32_16x16x32_bf16 v[36:39], v[84:87], v[172:175], v[36:39]\n\t" \
  "s_waitcnt vmcnt(0)\n\t" \
  "s_barrier\n\t" \
  "ds_read_b64_tr_b16 v[176:177], v7 offset:32768\n\t" \
  "ds_read_b64_tr_b16 v[178:179], v8 offset:32768\n\t" \
  "ds_read_b64_tr_b16 v[180:181], v9 offset:32768\n\t" \
  "ds_read_b64_tr_b16 v[182:183], v10 offset:32768\n\t" \
  "v_xor_b32 v120, 256, v13\n\t" \
  "ds_read_b128 v[208:211], v120\n\t" \
  "v_xor_b32 v121, 256, v14\n\t" \
  "ds_read_b128 v[212:215], v121\n\t" \
  "v_xor_b32 v122, 256, v15\n\t" \
  "ds_read_b128 v[216:219], v122\n\t" \
  "ds_read_b64_tr_b16 v[184:185], v7 offset:40960\n\t" \
  "ds_read_b64_tr_b16 v[186:187], v8 offset:40960\n\t" \
  "ds_read_b64_tr_b16 v[188:189], v9 offset:40960\n\t" \
  "ds_read_b64_tr_b16 v[190:191], v10 offset:40960\n\t" \
  "v_xor_b32 v120, 320, v13\n\t" \
  "ds_read_b128 v[220:223], v120\n\t" \
  "v_xor_b32 v121, 320, v14\n\t" \
  "ds_read_b128 v[224:227], v121\n\t" \
  "v_xor_b32 v122, 320, v15\n\t" \
  "ds_read_b128 v[228:231], v122\n\t" \
  "ds_read_b64_tr_b16 v[192:193], v7 offset:49152\n\t" \
  "ds_read_b64_tr_b16 v[194:195], v8 offset:49152\n\t" \
  "ds_read_b64_tr_b16 v[196:197], v9 offset:49152\n\t" \
  "ds_read_b64_tr_b16 v[198:199], v10 offset:49152\n\t" \
  "v_xor_b32 v120, 384, v13\n\t" \
  "ds_read_b128 v[232:235], v120\n\t" \
  "v_xor_b32 v121, 384, v14\n\t" \
  "ds_read_b128 v[236:239], v121\n\t" \
  "v_xor_b32 v122, 384, v15\n\t" \
  "ds_read_b128 v[240:243], v122\n\t" \
  "ds_read_b64_tr_b16 v[200:201], v7 offset:57344\n\t" \
  "ds_read_b64_tr_b16 v[202:203], v8 offset:57344\n\t" \
  "ds_read_b64_tr_b16 v[204:205], v9 offset:57344\n\t" \
  "ds_read_b64_tr_b16 v[206:207], v10 offset:57344\n\t" \
  "v_xor_b32 v120, 448, v13\n\t" \
  "ds_read_b128 v[244:247], v120\n\t" \
  "v_xor_b32 v121, 448, v14\n\t" \
  "ds_read_b128 v[248:251], v121\n\t" \
  "v_xor_b32 v122, 448, v15\n\t" \
  "ds_read_b128 v[252:255], v122\n\t" \
  "global_load_dwordx2 v[126:127], v110, s[66:67] sc1\n\t" \
  "s_waitcnt lgkmcnt(15)\n\t" \
  "v_mfma_f32_16x16x32_bf16 v[16:19], v[208:211], v[176:179], v[16:19]\n\t" \
  "v_lshlrev_b32 v118, 16, v96\n\t" \
  "v_and_b32 v119, 0xffff0000, v96\n\t" \
  "v_mfma_f32_16x16x32_bf16 v[20:23], v[208:211], v[180:183], v[20:23]\n\t" \
  "v_pk_mul_f32 v[118:119], v[118:119], s[68:69]\n\t" \
  "v_cvt_pk_bf16_f32 v88, v118, v119\n\t" \
  "s_waitcnt lgkmcnt(15)\n\t" \
  "v_mfma_f32_16x16x32_bf16 v[24:27], v[212:215], v[176:179], v[24:27]\n\t" \
  "v_lshlrev_b32 v118, 16, v97\n\t" \
  "v_and_b32 v119, 0xffff0000, v97\n\t" \
  "v_mfma_f32_16x16x32_bf16 v[28:31], v[212:215], v[180:183], v[28:31]\n\t" \
  "v_pk_mul_f32 v[118:119], v[118:119], s[68:69]\n\t" \
  "v_cvt_pk_bf16_f32 v89, v118, v119\n\t" \
  "s_waitcnt lgkmcnt(15)\n\t" \
  "v_mfma_f32_16x16x32_bf16 v[32:35], v[216:219], v[176:179], v[32:35]\n\t" \
  "v_lshlrev_b32 v118, 16, v98\n\t" \
  "v_and_b32 v119, 0xffff0000, v98\n\t" \
  "v_mfma_f32_16x16x32_bf16 v[36:39], v[216:219], v[180:183], v[36:39]\n\t" \
  "v_pk_mul_f32 v[118:119], v[118:119], s[68:69]\n\t" \
  "v_cvt_pk_bf16_f32 v90, v118, v119\n\t" \
  "s_waitcnt lgkmcnt(15)\n\t" \
  "v_mfma_f32_16x16x32_bf16 v[16:19], v[220:223], v[184:187], v[16:19]\n\t" \
  "v_lshlrev_b32 v118, 16, v99\n\t" \
  "v_and_b32 v119, 0xffff0000, v99\n\t" \
  "v_mfma_f32_16x16x32_bf16 v[20:23], v[220:223], v[188:191], v[20:23]\n\t" \
  "v_pk_mul_f32 v[118:119], v[118:119], s[68:69]\n\t" \
  "v_cvt_pk_bf16_f32 v91, v118, v119\n\t" \
  "s_waitcnt lgkmcnt(15)\n\t" \
  "v_mfma_f32_16x16x32_bf16 v[24:27], v[224:227], v[184:187], v[24:27]\n\t" \
  "v_lshlrev_b32 v128, 16, v100\n\t" \
  "v_and_b32 v129, 0xffff0000, v100\n\t" \
  "v_mfma_f32_16x16x32_bf16 v[28:31], v[224:227], v[188:191], v[28:31]\n\t" \
  "v_pk_mul_f32 v[128:129], v[128:129], s[70:71]\n\t" \
  "v_lshlrev_b32 v130, 16, v101\n\t" \
  "s_waitcnt lgkmcnt(14)\n\t" \
  "v_mfma_f32_16x16x32_bf16 v[32:35], v[228:231], v[184:187], v[32:35]\n\t" \
  "v_and_b32 v131, 0xffff0000, v101\n\t" \
  "v_pk_mul_f32 v[130:131], v[130:131], s[70:71]\n\t" \
  "v_mfma_f32_16x16x32_bf16 v[36:39], v[228:231], v[188:191], v[36:39]\n\t" \
  "v_lshlrev_b32 v132, 16, v102\n\t" \
  "v_and_b32 v133, 0xffff0000, v102\n\t" \
  "s_waitcnt lgkmcnt(9)\n\t" \
  "v_mfma_f32_16x16x32_bf16 v[16:19], v[232:235], v[192:195], v[16:19]\n\t" \
  "v_pk_mul_f32 v[132:133], v[132:133], s[70:71]\n\t" \
  "v_lshlrev_b32 v134, 16, v103\n\t" \
  "v_mfma_f32_16x16x32_bf16 v[20:23], v[232:235], v[196:199], v[20:23]\n\t" \
  "v_and_b32 v135, 0xffff0000, v103\n\t" \
  "v_pk_mul_f32 v[134:135], v[134:135], s[70:71]\n\t" \
  "s_waitcnt lgkmcnt(8)\n\t" \
  "v_mfma_f32_16x16x32_bf16 v[24:27], v[236:239], v[192:195], v[24:27]\n\t" \
  "v_lshlrev_b32 v136, 16, v104\n\t" \
  "v_and_b32 v137, 0xffff0000, v104\n\t" \
  "v_mfma_f32_16x16x32_bf16 v[28:31], v[236:239], v[196:199], v[28:31]\n\t" \
  "v_pk_mul_f32 v[136:137], v[136:137], s[70:71]\n\t" \
  "v_lshlrev_b32 v138, 16, v105\n\t" \
  "s_waitcnt lgkmcnt(7)\n\t" \
  "v_mfma_f32_16x16x32_bf16 v[32:35], v[240:243], v[192:195], v[32:35]\n\t" \
  "v_and_b32 v139, 0xffff0000, v105\n\t" \
  "v_pk_mul_f32 v[138:139], v[138:139], s[70:71]\n\t" \
  "v_mfma_f32_16x16x32_bf16 v[36:39], v[240:243], v[196:199], v[36:39]\n\t" \
  "v_lshlrev_b32 v140, 16, v106\n\t" \
  "v_and_b32 v141, 0xffff0000, v106\n\t" \
  "s_waitcnt lgkmcnt(2)\n\t" \
  "v_mfma_f32_16x16x32_bf16 v[16:19], v[244:247], v[200:203], v[16:19]\n\t" \
  "v_pk_mul_f32 v[140:141], v[140:141], s[70:71]\n\t" \
  "v_lshlrev_b32 v142, 16, v107\n\t" \
  "v_mfma_f32_16x16x32_bf16 v[20:23], v[244:247], v[204:207], v[20:23]\n\t" \
  "v_and_b32 v143, 0xffff0000, v107\n\t" \
  "v_pk_mul_f32 v[142:143], v[142:143], s[70:71]\n\t" \
  "s_waitcnt lgkmcnt(1)\n\t" \
  "v_mfma_f32_16x16x32_bf16 v[24:27], v[248:251], v[200:203], v[24:27]\n\t" \
  "v_lshlrev_b32 v116, 16, v124\n\t" \
  "v_mul_f32 v116, s68, v116\n\t" \
  "v_mfma_f32_16x16x32_bf16 v[28:31], v[248:251], v[204:207], v[28:31]\n\t" \
  "v_cvt_pk_bf16_f32 v117, v116, v116\n\t" \
  "v_and_b32 v117, 0xffff0000, v117\n\t" \
  "s_waitcnt lgkmcnt(0)\n\t" \
  "v_mfma_f32_16x16x32_bf16 v[32:35], v[252:255], v[200:203], v[32:35]\n\t" \
  "v_sub_f32 v116, v116, v117\n\t" \
  "v_cvt_pk_bf16_f32 v116, v116, v116\n\t" \
  "v_mfma_f32_16x16x32_bf16 v[36:39], v[252:255], v[204:207], v[36:39]\n\t" \
  "v_lshlrev_b32 v115, 31, v4\n\t" \
  "v_ashrrev_i32 v115, 15, v115\n\t" \
  "s_nop 3\n\t" \
  "v_pk_mul_f32 v[16:17], v[16:17], s[68:69]\n\t" \
  "v_pk_mul_f32 v[18:19], v[18:19], s[68:69]\n\t" \
  "v_pk_mul_f32 v[20:21], v[20:21], s[68:69]\n\t" \
  "v_pk_mul_f32 v[22:23], v[22:23], s[68:69]\n\t" \
  "v_pk_mul_f32 v[24:25], v[24:25], s[68:69]\n\t" \
  "v_pk_mul_f32 v[26:27], v[26:27], s[68:69]\n\t" \
  "v_pk_mul_f32 v[28:29], v[28:29], s[68:69]\n\t" \
  "v_pk_mul_f32 v[30:31], v[30:31], s[68:69]\n\t" \
  "v_pk_mul_f32 v[32:33], v[32:33], s[68:69]\n\t" \
  "v_pk_mul_f32 v[34:35], v[34:35], s[68:69]\n\t" \
  "v_pk_mul_f32 v[36:37], v[36:37], s[68:69]\n\t" \
  "v_pk_mul_f32 v[38:39], v[38:39], s[68:69]\n\t" \
  "v_cvt_pk_bf16_f32 v96, v16, v17\n\t" \
  "v_cvt_pk_bf16_f32 v97, v18, v19\n\t" \
  "v_cvt_pk_bf16_f32 v98, v20, v21\n\t" \
  "v_cvt_pk_bf16_f32 v99, v22, v23\n\t" \
  "v_cvt_pk_bf16_f32 v100, v24, v25\n\t" \
  "v_cvt_pk_bf16_f32 v101, v26, v27\n\t" \
  "v_cvt_pk_bf16_f32 v102, v28, v29\n\t" \
  "v_cvt_pk_bf16_f32 v103, v30, v31\n\t" \
  "v_cvt_pk_bf16_f32 v104, v32, v33\n\t" \
  "v_cvt_pk_bf16_f32 v105, v34, v35\n\t" \
  "v_cvt_pk_bf16_f32 v106, v36, v37\n\t" \
  "v_cvt_pk_bf16_f32 v107, v38, v39\n\t" \
  "s_nop 0\n\t" \
  "v_permlane16_swap_b32 v96, v98\n\t" \
  "v_permlane16_swap_b32 v97, v99\n\t" \
  "v_permlane16_swap_b32 v100, v102\n\t" \
  "v_permlane16_swap_b32 v101, v103\n\t" \
  "v_permlane16_swap_b32 v104, v106\n\t" \
  "v_permlane16_swap_b32 v105, v107\n\t" \
  "s_nop 0\n\t" \
  "v_bfi_b32 v97, v115, v117, v97\n\t" \
  "v_bfi_b32 v99, v115, v116, v99\n\t" \
  "v_bfi_b32 v101, v115, v117, v101\n\t" \
  "v_bfi_b32 v103, v115, v116, v103\n\t" \
  "v_bfi_b32 v105, v115, v117, v105\n\t" \
  "v_bfi_b32 v107, v115, v116, v107\n\t" \
  "v_mov_b64 v[108:109], v[104:105]\n\t" \
  "s_waitcnt vmcnt(0)\n\t" \
  ""
#define EDGE_LOOP_ASM \
  "v_lshlrev_b32 v144, 1, v111\n\t" \
  "v_mov_b64 v[0:1], 0\n\t" \
  "v_mov_b64 v[2:3], 0\n\t" \
  "v_mov_b64 v[4:5], 0\n\t" \
  "v_mov_b32 v6, 0\n\t" \
  "s_mov_b32 exec_hi, 0xff\n\t" \
  "global_load_dwordx4 v[0:3], v144, s[80:81] sc1\n\t" \
  "global_load_dwordx3 v[4:6], v144, s[80:81] offset:16 sc1\n\t" \
  "s_mov_b32 exec_hi, -1\n\t" \
  "v_lshrrev_b32 v24, 4, v111\n\t" \
  "v_mul_u32_u24 v27, 43, v24\n\t" \
  "v_lshrrev_b32 v27, 9, v27\n\t" \
  "v_min_u32 v27, 4, v27\n\t" \
  "v_mad_i32_i24 v28, v27, -12, v24\n\t" \
  "s_mul_i32 s79, s61, 5\n\t" \
  "v_add_u32 v29, s79, v27\n\t" \
  "v_mul_u32_u24 v149, 3, v28\n\t" \
  "v_lshlrev_b32 v145, 8, v29\n\t" \
  "v_lshl_add_u32 v145, v149, 2, v145\n\t" \
  "global_load_dwordx3 v[8:10], v145, s[82:83] sc1\n\t" \
  "v_mov_b32 v22, 1048576\n\t" \
  "v_cmp_gt_u32 vcc, 60, v24\n\t" \
  "v_lshrrev_b32 v25, 5, v24\n\t" \
  "v_and_b32 v26, 31, v24\n\t" \
  "v_cndmask_b32 v149, v22, v149, vcc\n\t" \
  "v_subrev_u32 v30, s79, v24\n\t" \
  "v_cmp_gt_u32 vcc, 5, v30\n\t" \
  "v_add_u32 v146, 139936, v111\n\t" \
  "v_lshlrev_b32 v147, 4, v29\n\t" \
  "v_cndmask_b32 v146, v22, v146, vcc\n\t" \
  "v_add_u32 v147, 139936, v147\n\t" \
  "v_mul_u32_u24 v148, 4000, v29\n\t" \
  "v_and_b32 v151, 0x1f0, v111\n\t" \
  "v_lshlrev_b32 v155, 4, v25\n\t" \
  "v_lshlrev_b32 v159, 20, v25\n\t" \
  "v_and_b32 v30, 3, v24\n\t" \
  "v_lshlrev_b32 v30, 1, v30\n\t" \
  "v_and_or_b32 v30, v24, 8, v30\n\t" \
  "v_lshlrev_b32 v31, 6, v24\n\t" \
  "v_and_b32 v31, 0x400, v31\n\t" \
  "v_lshlrev_b32 v150, 7, v24\n\t" \
  "v_and_b32 v150, 0x200, v150\n\t" \
  "v_or3_b32 v150, v150, v30, v31\n\t" \
  "s_lshl_b32 s79, s61, 8\n\t" \
  "s_add_u32 s79, s79, 133792\n\t" \
  "v_lshl_add_u32 v154, v26, 3, s79\n\t" \
  "v_lshl_add_u32 v152, v25, 11, v154\n\t" \
  "s_and_b32 s78, s61, 3\n\t" \
  "s_lshl_b32 s79, s78, 9\n\t" \
  "s_add_u32 s79, s79, 137888\n\t" \
  "v_lshl_add_u32 v153, v24, 3, s79\n\t" \
  "v_lshlrev_b32 v156, 16, v151\n\t" \
  "v_add_u32 v156, 33947680, v156\n\t" \
  "s_lshl_b32 s79, s61, 10\n\t" \
  "s_add_u32 s79, s79, 1188512\n\t" \
  "v_add_u32 v157, s79, v151\n\t" \
  "v_sub_u32 v157, v157, v159\n\t" \
  "s_lshl_b32 s79, s78, 11\n\t" \
  "s_add_u32 s79, s79, 148128\n\t" \
  "v_add_u32 v158, s79, v151\n\t" \
  "v_lshl_add_u32 v158, v25, 10, v158\n\t" \
  "v_lshl_add_u32 v30, v25, 3, s61\n\t" \
  "v_bfm_b32 v160, v30, 0\n\t" \
  "s_lshl1_add_u32 s79, s78, 16\n\t" \
  "v_add_u32 v30, s79, v25\n\t" \
  "v_bfm_b32 v161, v30, 0\n\t" \
  "v_lshlrev_b32 v30, 7, v25\n\t" \
  "v_xor_b32 v113, v30, v151\n\t" \
  "s_lshl_b32 s79, s61, 5\n\t" \
  "s_add_u32 s79, s79, 134820\n\t" \
  "v_lshl_add_u32 v112, v25, 3, s79\n\t" \
  "v_lshrrev_b32 v30, 1, v26\n\t" \
  "v_mul_u32_u24 v110, 1040, v30\n\t" \
  "v_and_b32 v30, 1, v24\n\t" \
  "v_mul_u32_u24 v30, 640, v30\n\t" \
  "s_lshl_b32 s79, s61, 6\n\t" \
  "v_xor_b32 v30, s79, v30\n\t" \
  "v_add3_u32 v110, v110, v30, v155\n\t" \
  "v_lshrrev_b32 v30, 2, v26\n\t" \
  "v_mul_u32_u24 v30, 528, v30\n\t" \
  "s_add_u32 s79, s79, 111616\n\t" \
  "v_lshl_add_u32 v30, v25, 3, v30\n\t" \
  "v_add_u32 v30, s79, v30\n\t" \
  "s_mov_b32 vcc_lo, 0x01111111\n\t" \
  "s_mov_b32 vcc_hi, 0x01111111\n\t" \
  "v_cndmask_b32 v120, v22, v30, vcc\n\t" \
  "s_lshl_b32 s79, s61, 8\n\t" \
  "s_add_u32 s79, s79, 99328\n\t" \
  "v_lshl_add_u32 v121, v24, 2, s79\n\t" \
  "v_add_u32 v122, 101376, v111\n\t" \
  "s_xor_b32 s79, s61, 4\n\t" \
  "s_lshl_b32 s79, s79, 10\n\t" \
  "s_add_u32 s79, s79, 139936\n\t" \
  "v_add_u32 v123, s79, v111\n\t" \
  "v_lshlrev_b32 v30, 3, v25\n\t" \
  "v_sub_u32 v30, v26, v30\n\t" \
  "v_and_b32 v31, 1, v24\n\t" \
  "v_lshlrev_b32 v31, 4, v31\n\t" \
  "s_movk_i32 s79, 0x3f80\n\t" \
  "v_lshlrev_b32_e64 v31, v31, s79\n\t" \
  "v_ashrrev_i32 v30, 1, v30\n\t" \
  "v_cmp_eq_u32 vcc, 0, v30\n\t" \
  "s_nop 0\n\t" \
  "v_cmp_eq_u32 s[86:87], 1, v30\n\t" \
  "v_cmp_eq_u32 s[44:45], 2, v30\n\t" \
  "v_cndmask_b32 v80, 0, v31, vcc\n\t" \
  "v_cmp_eq_u32 vcc, 3, v30\n\t" \
  "v_cndmask_b32 v81, 0, v31, s[86:87]\n\t" \
  "v_cmp_eq_u32 s[86:87], 8, v30\n\t" \
  "v_cndmask_b32 v82, 0, v31, s[44:45]\n\t" \
  "v_cmp_eq_u32 s[44:45], 9, v30\n\t" \
  "v_cndmask_b32 v83, 0, v31, vcc\n\t" \
  "v_cmp_eq_u32 vcc, 10, v30\n\t" \
  "v_cndmask_b32 v84, 0, v31, s[86:87]\n\t" \
  "v_cmp_eq_u32 s[86:87], 11, v30\n\t" \
  "v_cndmask_b32 v85, 0, v31, s[44:45]\n\t" \
  "v_cndmask_b32 v86, 0, v31, vcc\n\t" \
  "s_nop 0\n\t" \
  "v_cndmask_b32 v87, 0, v31, s[86:87]\n\t" \
  "s_lshl_b32 s79, s61, 10\n\t" \
  "s_add_u32 s79, s79, 66560\n\t" \
  "v_add_u32 v16, s79, v111\n\t" \
  "v_mov_b64 v[12:13], 0\n\t" \
  "v_mov_b64 v[14:15], 0\n\t" \
  "ds_write_b128 v16, v[12:15] offset:0\n\t" \
  "ds_write_b128 v16, v[12:15] offset:8192\n\t" \
  "ds_write_b128 v16, v[12:15] offset:16384\n\t" \
  "ds_write_b128 v16, v[12:15] offset:24576\n\t" \
  "ds_write_b128 v16, v[12:15] offset:32768\n\t" \
  "ds_write_b128 v16, v[12:15] offset:40960\n\t" \
  "s_mov_b32 s77, 0\n\t" \
  "v_add_u32 v20, 1, v149\n\t" \
  "v_add_u32 v21, 2, v149\n\t" \
  "s_mov_b32 s67, 133792\n\t" \
  "s_waitcnt vmcnt(0)\n\t" \
  "s_andn2_b32 s78, s60, 7\n\t" \
  "s_add_u32 s78, s78, s61\n\t" \
  "s_mul_i32 s78, s78, 3072\n\t" \
  "v_add_u32 v31, s78, v111\n\t" \
  "global_load_dwordx4 v[192:195], v31, s[52:53] offset:0 sc1\n\t" \
  "global_load_dwordx4 v[196:199], v31, s[52:53] offset:1024 sc1\n\t" \
  "global_load_dwordx4 v[200:203], v31, s[52:53] offset:2048 sc1\n\t" \
  "v_add_u32_dpp v12, v0, v0 row_shr:1 row_mask:0xf bank_mask:0xf bound_ctrl:1\n\t" \
  "v_add_u32_dpp v13, v1, v1 row_shr:1 row_mask:0xf bank_mask:0xf bound_ctrl:1\n\t" \
  "v_add_u32_dpp v14, v2, v2 row_shr:1 row_mask:0xf bank_mask:0xf bound_ctrl:1\n\t" \
  "v_add_u32_dpp v12, v12, v12 row_shr:2 row_mask:0xf bank_mask:0xf bound_ctrl:1\n\t" \
  "v_add_u32_dpp v13, v13, v13 row_shr:2 row_mask:0xf bank_mask:0xf bound_ctrl:1\n\t" \
  "v_add_u32_dpp v14, v14, v14 row_shr:2 row_mask:0xf bank_mask:0xf bound_ctrl:1\n\t" \
  "v_add_u32_dpp v12, v12, v12 row_shr:4 row_mask:0xf bank_mask:0xf bound_ctrl:1\n\t" \
  "v_add_u32_dpp v13, v13, v13 row_shr:4 row_mask:0xf bank_mask:0xf bound_ctrl:1\n\t" \
  "v_add_u32_dpp v14, v14, v14 row_shr:4 row_mask:0xf bank_mask:0xf bound_ctrl:1\n\t" \
  "v_add_u32_dpp v12, v12, v12 row_shr:8 row_mask:0xf bank_mask:0xf bound_ctrl:1\n\t" \
  "v_add_u32_dpp v13, v13, v13 row_shr:8 row_mask:0xf bank_mask:0xf bound_ctrl:1\n\t" \
  "v_add_u32_dpp v14, v14, v14 row_shr:8 row_mask:0xf bank_mask:0xf bound_ctrl:1\n\t" \
  "v_add_u32_dpp v12, v12, v12 row_bcast:15 row_mask:0xa bank_mask:0xf\n\t" \
  "v_add_u32_dpp v13, v13, v13 row_bcast:15 row_mask:0xa bank_mask:0xf\n\t" \
  "v_add_u32_dpp v14, v14, v14 row_bcast:15 row_mask:0xa bank_mask:0xf\n\t" \
  "v_add_u32_dpp v12, v12, v12 row_bcast:31 row_mask:0xc bank_mask:0xf\n\t" \
  "v_add_u32_dpp v13, v13, v13 row_bcast:31 row_mask:0xc bank_mask:0xf\n\t" \
  "v_add_u32_dpp v14, v14, v14 row_bcast:31 row_mask:0xc bank_mask:0xf\n\t" \
  "v_readlane_b32 s68, v12, 63\n\t" \
  "v_readlane_b32 s69, v13, 63\n\t" \
  "v_readlane_b32 s70, v14, 63\n\t" \
  "v_cmp_lt_u32 vcc, 48, v3\n\t" \
  "s_and_b32 s71, s68, 0xffff\n\t" \
  "s_lshr_b32 s79, s68, 16\n\t" \
  "s_add_u32 s72, s71, s79\n\t" \
  "s_and_b32 s79, s69, 0xffff\n\t" \
  "s_add_u32 s73, s72, s79\n\t" \
  "s_lshr_b32 s79, s69, 16\n\t" \
  "s_add_u32 s74, s73, s79\n\t" \
  "s_and_b32 s79, s70, 0xffff\n\t" \
  "s_add_u32 s75, s74, s79\n\t" \
  "s_lshr_b32 s79, s70, 16\n\t" \
  "s_add_u32 s76, s75, s79\n\t" \
  "s_lshl_b32 s68, s71, 16\n\t" \
  "s_pack_ll_b32_b16 s69, s72, s73\n\t" \
  "s_pack_ll_b32_b16 s70, s74, s75\n\t" \
  "s_cbranch_vccnz SLOW_%=\n\t" \
  "s_waitcnt vmcnt(3)\n\t" \
  "v_pk_sub_u16 v0, v12, v4\n\t" \
  "v_pk_sub_u16 v1, v13, v5\n\t" \
  "v_pk_sub_u16 v2, v14, v6\n\t" \
  "v_pk_add_u16 v0, v0, s68\n\t" \
  "v_pk_add_u16 v1, v1, s69\n\t" \
  "v_pk_add_u16 v2, v2, s70\n\t" \
  "ds_write_b128 v146, v[0:3]\n\t" \
  "ds_read_b32 v7, v147 offset:12\n\t" \
  "s_waitcnt vmcnt(3)\n\t" \
  "v_lshrrev_b32 v12, 28, v8\n\t" \
  "v_and_or_b32 v12, v12, 14, v147\n\t" \
  "ds_read_u16 v12, v12\n\t" \
  "v_lshrrev_b32 v13, 28, v9\n\t" \
  "v_and_or_b32 v13, v13, 14, v147\n\t" \
  "ds_read_u16 v13, v13\n\t" \
  "v_lshrrev_b32 v14, 28, v10\n\t" \
  "v_and_or_b32 v14, v14, 14, v147\n\t" \
  "ds_read_u16 v14, v14\n\t" \
  "s_sub_u32 s79, s71, 1\n\t" \
  "s_lshr_b32 s79, s79, 5\n\t" \
  "s_bitset1_b32 s77, s79\n\t" \
  "s_sub_u32 s79, s72, 1\n\t" \
  "s_lshr_b32 s79, s79, 5\n\t" \
  "s_bitset1_b32 s77, s79\n\t" \
  "s_sub_u32 s79, s73, 1\n\t" \
  "s_lshr_b32 s79, s79, 5\n\t" \
  "s_bitset1_b32 s77, s79\n\t" \
  "s_sub_u32 s79, s74, 1\n\t" \
  "s_lshr_b32 s79, s79, 5\n\t" \
  "s_bitset1_b32 s77, s79\n\t" \
  "s_sub_u32 s79, s75, 1\n\t" \
  "s_lshr_b32 s79, s79, 5\n\t" \
  "s_bitset1_b32 s77, s79\n\t" \
  "s_cmp_eq_u32 s71, 0\n\t" \
  "s_cbranch_scc1 SLOW_%=\n\t" \
  "s_sub_u32 s79, s76, 225\n\t" \
  "s_cmp_ge_u32 s79, 544\n\t" \
  "s_cbranch_scc1 SLOW_%=\n\t" \
  "s_lshl_b32 s42, s77, 1\n\t" \
  "s_and_b32 s79, s42, s77\n\t" \
  "s_cbranch_scc1 SLOW_%=\n\t" \
  "s_bcnt1_i32_b32 s79, s77\n\t" \
  "s_cmp_lg_u32 s79, 5\n\t" \
  "s_cbranch_scc1 SLOW_%=\n\t" \
  "s_add_u32 s78, s76, 31\n\t" \
  "s_lshr_b32 s78, s78, 5\n\t" \
  "s_waitcnt lgkmcnt(0)\n\t" \
  "v_cmp_lt_u32 vcc, v149, v7\n\t" \
  "v_add_u16 v12, v12, v149\n\t" \
  "v_lshl_add_u32 v12, v12, 3, s67\n\t" \
  "v_cndmask_b32 v12, v22, v12, vcc\n\t" \
  "ds_write2_b32 v12, v8, v148 offset1:1\n\t" \
  "v_cmp_lt_u32 vcc, v20, v7\n\t" \
  "v_add_u16 v13, v13, v20\n\t" \
  "v_lshl_add_u32 v13, v13, 3, s67\n\t" \
  "v_cndmask_b32 v13, v22, v13, vcc\n\t" \
  "ds_write2_b32 v13, v9, v148 offset1:1\n\t" \
  "v_cmp_lt_u32 vcc, v21, v7\n\t" \
  "v_add_u16 v14, v14, v21\n\t" \
  "v_lshl_add_u32 v14, v14, 3, s67\n\t" \
  "v_cndmask_b32 v14, v22, v14, vcc\n\t" \
  "ds_write2_b32 v14, v10, v148 offset1:1\n\t" \
  "v_cmp_lt_u32 vcc, 36, v7\n\t" \
  "s_cbranch_vccnz MORE_%=\n\t" \
  "BACK_%=:\n\t" \
  "s_cmp_lg_u32 s61, 7\n\t" \
  "s_cbranch_scc1 NOPAD_%=\n\t" \
  "v_lshrrev_b32 v12, 4, v111\n\t" \
  "v_add_u32 v12, s76, v12\n\t" \
  "s_lshl_b32 s79, s78, 5\n\t" \
  "v_cmp_gt_u32 vcc, s79, v12\n\t" \
  "v_lshl_add_u32 v12, v12, 3, s67\n\t" \
  "v_mov_b32 v13, 0xfc000000\n\t" \
  "v_mov_b32 v14, 0\n\t" \
  "v_cndmask_b32 v12, v22, v12, vcc\n\t" \
  "ds_write2_b32 v12, v13, v14 offset1:1\n\t" \
  "NOPAD_%=:\n\t" \
  "s_sub_u32 s37, s78, 4\n\t" \
  "s_and_b32 s47, s37, 1\n\t" \
  "s_bfm_b32 s71, s78, 0\n\t" \
  "v_subrev_u32 v23, 1028, v112\n\t" \
  "s_waitcnt lgkmcnt(0)\n\t" \
  "s_barrier\n\t" \
  "ds_read_b64 v[18:19], v153\n\t" \
  "ds_read_b64 v[16:17], v152\n\t" \
  "ds_read2_b32 v[24:25], v23 offset0:0 offset1:4\n\t" \
  "ds_read_b64 v[20:21], v154\n\t" \
  "ds_read2_b32 v[26:27], v23 offset0:64 offset1:68\n\t" \
  "ds_read2_b32 v[28:29], v23 offset0:128 offset1:132\n\t" \
  "ds_read2_b32 v[30:31], v23 offset0:192 offset1:196\n\t" \
  "s_add_u32 s79, s47, 3\n\t" \
  "s_and_b32 s79, s79, 3\n\t" \
  "s_mul_i32 s72, s79, 16640\n\t" \
  "s_add_u32 s79, s47, 4\n\t" \
  "s_and_b32 s79, s79, 3\n\t" \
  "s_mul_i32 s73, s79, 16640\n\t" \
  "s_add_u32 s79, s47, 5\n\t" \
  "s_and_b32 s79, s79, 3\n\t" \
  "s_mul_i32 s74, s79, 16640\n\t" \
  "s_add_u32 s79, s47, 6\n\t" \
  "s_and_b32 s79, s79, 3\n\t" \
  "s_mul_i32 s75, s79, 16640\n\t" \
  "s_add_u32 s79, s61, s47\n\t" \
  "s_add_u32 s68, s79, 7\n\t" \
  "s_and_b32 s68, s68, 7\n\t" \
  "s_lshl_b32 s68, s68, 12\n\t" \
  "s_add_u32 s68, s68, 66560\n\t" \
  "s_and_b32 s69, s79, 7\n\t" \
  "s_lshl_b32 s69, s69, 12\n\t" \
  "s_add_u32 s69, s69, 68608\n\t" \
  "v_mov_b32 v36, 0xfc000000\n\t" \
  "s_movk_i32 s70, 0x3fff\n\t" \
  "s_lshl1_add_u32 s76, s61, 16\n\t" \
  "s_cmp_ge_u32 s76, s78\n\t" \
  "s_cbranch_scc1 NOB1_%=\n\t" \
  "s_waitcnt lgkmcnt(6)\n\t" \
  "v_cmp_gt_u32 vcc, s71, v161\n\t" \
  "s_nop 1\n\t" \
  "v_cndmask_b32 v18, v36, v18, vcc\n\t" \
  "v_and_b32 v39, s70, v18\n\t" \
  "v_mul_u32_u24 v34, 6, v39\n\t" \
  "v_and_b32 v34, -4, v34\n\t" \
  "global_load_dwordx2 v[34:35], v34, s[84:85]\n\t" \
  "NOB1_%=:\n\t" \
  "s_waitcnt lgkmcnt(5)\n\t" \
  "v_cmp_gt_u32 vcc, s71, v160\n\t" \
  "s_nop 1\n\t" \
  "v_cndmask_b32 v16, v36, v16, vcc\n\t" \
  "v_and_b32 v38, s70, v16\n\t" \
  "v_mul_u32_u24 v32, 6, v38\n\t" \
  "v_and_b32 v32, -4, v32\n\t" \
  "global_load_dwordx2 v[32:33], v32, s[84:85]\n\t" \
  "s_waitcnt lgkmcnt(3)\n\t" \
  "v_bfe_u32 v20, v20, 14, 12\n\t" \
  "v_add_lshl_u32 v20, v20, v21, 5\n\t" \
  "v_or_b32 v20, v20, v155\n\t" \
  "s_cmp_eq_u32 s61, 0\n\t" \
  "s_cbranch_scc0 NE0_%=\n\t" \
  "s_add_u32 m0, s68, 1024\n\t" \
  "s_nop 0\n\t" \
  "global_load_lds_dwordx4 v20, s[54:55]\n\t" \
  "NE0_%=:\n\t" \
  "v_and_b32 v24, s70, v24\n\t" \
  "s_add_u32 m0, s72, s39\n\t" \
  "v_lshl_or_b32 v24, v24, 9, v113\n\t" \
  "global_load_lds_dwordx4 v24, s[52:53] sc1\n\t" \
  "v_and_b32 v25, s70, v25\n\t" \
  "s_add_u32 m0, m0, 1040\n\t" \
  "v_lshl_or_b32 v25, v25, 9, v113\n\t" \
  "global_load_lds_dwordx4 v25, s[52:53] sc1\n\t" \
  "s_waitcnt lgkmcnt(0)\n\t" \
  "s_cmp_eq_u32 s61, 1\n\t" \
  "s_cbranch_scc0 NE1_%=\n\t" \
  "s_add_u32 m0, s68, 1024\n\t" \
  "s_nop 0\n\t" \
  "global_load_lds_dwordx4 v20, s[54:55]\n\t" \
  "NE1_%=:\n\t" \
  "v_and_b32 v26, s70, v26\n\t" \
  "s_add_u32 m0, s73, s39\n\t" \
  "v_lshl_or_b32 v26, v26, 9, v113\n\t" \
  "global_load_lds_dwordx4 v26, s[52:53] sc1\n\t" \
  "v_and_b32 v27, s70, v27\n\t" \
  "s_add_u32 m0, m0, 1040\n\t" \
  "v_lshl_or_b32 v27, v27, 9, v113\n\t" \
  "global_load_lds_dwordx4 v27, s[52:53] sc1\n\t" \
  "s_mov_b32 s66, 160768\n\t" \
  "s_movk_i32 s67, 0x3f80\n\t" \
  "s_mov_b32 s79, 101376\n\t" \
  "s_mov_b32 s65, 0x07060302\n\t" \
  "v_lshrrev_b32 v55, 26, v16\n\t" \
  "v_lshl_add_u32 v56, v55, 4, s66\n\t" \
  "ds_read_b96 v[48:50], v56\n\t" \
  "v_lshlrev_b32 v56, 9, v38\n\t" \
  "v_bfe_u32 v43, v16, 14, 12\n\t" \
  "v_add_lshl_u32 v43, v43, v17, 5\n\t" \
  "ds_write_b32 v152, v56 offset:4\n\t" \
  "v_or_b32 v47, 16, v43\n\t" \
  "v_lshrrev_b32 v54, 29, v16\n\t" \
  "v_and_b32 v55, 7, v55\n\t" \
  "v_lshl_add_u32 v46, v55, 6, v150\n\t" \
  "v_lshrrev_b32 v53, 2, v55\n\t" \
  "v_and_b32 v55, 3, v55\n\t" \
  "v_lshl_add_u32 v53, v53, 9, v151\n\t" \
  "v_lshl_add_u32 v53, v55, 1, v53\n\t" \
  "v_lshl_or_b32 v42, v53, 16, v46\n\t" \
  "v_and_b32 v55, s42, v160\n\t" \
  "v_bcnt_u32_b32 v55, v55, 0\n\t" \
  "v_sub_u32 v55, v54, v55\n\t" \
  "v_cmp_gt_u32 vcc, 2, v55\n\t" \
  "v_lshlrev_b32 v55, 4, v55\n\t" \
  "v_lshlrev_b32_e64 v55, v55, s67\n\t" \
  "v_and_b32 v56, s77, v160\n\t" \
  "v_cndmask_b32 v41, 0, v55, vcc\n\t" \
  "v_bcnt_u32_b32 v56, v56, 0\n\t" \
  "v_sub_u32 v55, v54, v56\n\t" \
  "v_cmp_eq_u32 vcc, 1, v55\n\t" \
  "v_cmp_eq_u32 s[44:45], 0, v55\n\t" \
  "v_lshl_add_u32 v56, v56, 11, s79\n\t" \
  "v_cndmask_b32 v52, v22, v56, vcc\n\t" \
  "v_add_u32 v56, s69, v159\n\t" \
  "v_add3_u32 v53, v53, s68, v159\n\t" \
  "v_cndmask_b32 v52, v52, v56, s[44:45]\n\t" \
  "v_add_u32 v52, v52, v46\n\t" \
  "v_add_u32 v46, v46, v156\n\t" \
  "s_cmp_ge_u32 s76, s78\n\t" \
  "s_cbranch_scc1 NOB2_%=\n\t" \
  "v_lshrrev_b32 v179, 26, v18\n\t" \
  "v_lshl_add_u32 v180, v179, 4, s66\n\t" \
  "ds_read_b96 v[172:174], v180\n\t" \
  "v_lshlrev_b32 v180, 9, v39\n\t" \
  "v_bfe_u32 v167, v18, 14, 12\n\t" \
  "v_add_lshl_u32 v167, v167, v19, 5\n\t" \
  "ds_write_b32 v153, v180 offset:4\n\t" \
  "v_or_b32 v171, 16, v167\n\t" \
  "v_lshrrev_b32 v178, 29, v18\n\t" \
  "v_and_b32 v179, 7, v179\n\t" \
  "v_lshl_add_u32 v170, v179, 6, v150\n\t" \
  "v_lshrrev_b32 v177, 2, v179\n\t" \
  "v_and_b32 v179, 3, v179\n\t" \
  "v_lshl_add_u32 v177, v177, 9, v151\n\t" \
  "v_lshl_add_u32 v177, v179, 1, v177\n\t" \
  "v_lshl_or_b32 v166, v177, 16, v170\n\t" \
  "v_and_b32 v179, s42, v161\n\t" \
  "v_bcnt_u32_b32 v179, v179, 0\n\t" \
  "v_sub_u32 v179, v178, v179\n\t" \
  "v_cmp_gt_u32 vcc, 2, v179\n\t" \
  "v_lshlrev_b32 v179, 4, v179\n\t" \
  "v_lshlrev_b32_e64 v179, v179, s67\n\t" \
  "v_and_b32 v180, s77, v161\n\t" \
  "v_cndmask_b32 v165, 0, v179, vcc\n\t" \
  "v_bcnt_u32_b32 v180, v180, 0\n\t" \
  "v_sub_u32 v179, v178, v180\n\t" \
  "v_cmp_eq_u32 vcc, 1, v179\n\t" \
  "v_cmp_eq_u32 s[86:87], 0, v179\n\t" \
  "v_lshl_add_u32 v180, v180, 11, s79\n\t" \
  "v_cndmask_b32 v176, v22, v180, vcc\n\t" \
  "v_add_u32 v176, v176, v170\n\t" \
  "v_add_u32 v170, v170, v156\n\t" \
  "s_waitcnt vmcnt(5) lgkmcnt(0)\n\t" \
  "v_lshlrev_b32 v179, 4, v18\n\t" \
  "v_alignbit_b32 v34, v35, v34, v179\n\t" \
  "v_lshrrev_b32 v35, v179, v35\n\t" \
  "v_lshlrev_b32 v180, 16, v34\n\t" \
  "v_and_b32 v181, 0xffff0000, v34\n\t" \
  "v_lshlrev_b32 v182, 16, v35\n\t" \
  "v_pk_add_f32 v[180:181], v[172:173], v[180:181] neg_lo:[0,1] neg_hi:[0,1]\n\t" \
  "v_sub_f32 v182, v174, v182\n\t" \
  "v_cvt_pk_bf16_f32 v34, v180, v181\n\t" \
  "v_cvt_pk_bf16_f32 v182, v182, v182\n\t" \
  "v_lshlrev_b32 v180, 16, v34\n\t" \
  "v_and_b32 v181, 0xffff0000, v34\n\t" \
  "v_lshlrev_b32 v182, 16, v182\n\t" \
  "v_pk_mul_f32 v[180:181], v[180:181], v[180:181]\n\t" \
  "v_mul_f32 v183, v182, v182\n\t" \
  "v_cvt_pk_bf16_f32 v180, v180, v181\n\t" \
  "v_cvt_pk_bf16_f32 v183, v183, v183\n\t" \
  "v_and_b32 v181, 0xffff0000, v180\n\t" \
  "v_lshlrev_b32 v180, 16, v180\n\t" \
  "v_lshlrev_b32 v183, 16, v183\n\t" \
  "v_add_f32 v180, v180, v181\n\t" \
  "v_lshl_or_b32 v184, v34, 16, s67\n\t" \
  "v_add_f32 v180, v180, v183\n\t" \
  "v_perm_b32 v185, v182, v34, s65\n\t" \
  "v_cvt_pk_bf16_f32 v169, v180, v180\n\t" \
  "v_cndmask_b32 v164, 0, v184, s[86:87]\n\t" \
  "v_cndmask_b32 v168, 0, v185, s[86:87]\n\t" \
  "ds_write_b16 v176, v184\n\t" \
  "ds_write_b16_d16_hi v176, v184 offset:16\n\t" \
  "ds_write_b16 v176, v185 offset:32\n\t" \
  "ds_write_b16_d16_hi v176, v185 offset:48\n\t" \
  "ds_write_b128 v158, v[164:167]\n\t" \
  "ds_write_b128 v158, v[168:171] offset:512\n\t" \
  "NOB2_%=:\n\t" \
  "s_waitcnt vmcnt(4) lgkmcnt(0)\n\t" \
  "v_lshlrev_b32 v55, 4, v16\n\t" \
  "v_alignbit_b32 v32, v33, v32, v55\n\t" \
  "v_lshrrev_b32 v33, v55, v33\n\t" \
  "v_lshlrev_b32 v56, 16, v32\n\t" \
  "v_and_b32 v57, 0xffff0000, v32\n\t" \
  "v_lshlrev_b32 v58, 16, v33\n\t" \
  "v_pk_add_f32 v[56:57], v[48:49], v[56:57] neg_lo:[0,1] neg_hi:[0,1]\n\t" \
  "v_sub_f32 v58, v50, v58\n\t" \
  "v_cvt_pk_bf16_f32 v32, v56, v57\n\t" \
  "v_cvt_pk_bf16_f32 v58, v58, v58\n\t" \
  "v_lshlrev_b32 v56, 16, v32\n\t" \
  "v_and_b32 v57, 0xffff0000, v32\n\t" \
  "v_lshlrev_b32 v58, 16, v58\n\t" \
  "v_pk_mul_f32 v[56:57], v[56:57], v[56:57]\n\t" \
  "v_mul_f32 v59, v58, v58\n\t" \
  "v_cvt_pk_bf16_f32 v56, v56, v57\n\t" \
  "v_cvt_pk_bf16_f32 v59, v59, v59\n\t" \
  "v_and_b32 v57, 0xffff0000, v56\n\t" \
  "v_lshlrev_b32 v56, 16, v56\n\t" \
  "v_lshlrev_b32 v59, 16, v59\n\t" \
  "v_add_f32 v56, v56, v57\n\t" \
  "v_lshl_or_b32 v60, v32, 16, s67\n\t" \
  "v_add_f32 v56, v56, v59\n\t" \
  "v_perm_b32 v61, v58, v32, s65\n\t" \
  "v_cvt_pk_bf16_f32 v45, v56, v56\n\t" \
  "v_cndmask_b32 v40, 0, v60, s[44:45]\n\t" \
  "v_cndmask_b32 v44, 0, v61, s[44:45]\n\t" \
  "ds_write_b16 v52, v60\n\t" \
  "ds_write_b16_d16_hi v52, v60 offset:16\n\t" \
  "ds_write_b16 v52, v61 offset:32\n\t" \
  "ds_write_b16_d16_hi v52, v61 offset:48\n\t" \
  "ds_write_b128 v157, v[40:43]\n\t" \
  "ds_write_b128 v157, v[44:47] offset:512\n\t" \
  "v_add3_u32 v55, v151, s68, v159\n\t" \
  "ds_write_b16 v53, v41\n\t" \
  "ds_write_b16_d16_hi v53, v41 offset:8\n\t" \
  "ds_write_b16 v55, v45 offset:518\n\t" \
  "ds_write_b16 v55, v45 offset:526\n\t" \
  "s_and_b32 s79, s61, 3\n\t" \
  "s_sub_u32 s79, s37, s79\n\t" \
  "s_bfm_b32 s79, s79, 0\n\t" \
  "s_and_b32 s40, s40, s79\n\t" \
  "s_or_b32 s41, s40, s42\n\t" \
  "s_bitset1_b32 s41, s37\n\t" \
  "s_cmp_eq_u32 s47, 0\n\t" \
  "s_cselect_b32 s38, s72, s73\n\t" \
  "s_add_u32 s38, s38, s39\n\t" \
  "s_add_u32 s79, s61, s47\n\t" \
  "s_add_u32 s45, s79, 4\n\t" \
  "s_and_b32 s45, s45, 7\n\t" \
  "s_lshl_b32 s45, s45, 12\n\t" \
  "s_add_u32 s45, s45, 66560\n\t" \
  "s_add_u32 s46, s79, 3\n\t" \
  "s_and_b32 s46, s46, 7\n\t" \
  "s_lshl_b32 s46, s46, 12\n\t" \
  "s_add_u32 s46, s46, 66560\n\t" \
  "s_mov_b32 s64, s72\n\t" \
  "s_add_u32 s65, s47, 7\n\t" \
  "s_and_b32 s65, s65, 7\n\t" \
  "s_lshl_b32 s65, s65, 12\n\t" \
  "s_add_u32 s65, s65, 66560\n\t" \
  "s_waitcnt vmcnt(2) lgkmcnt(0)\n\t" \
  "s_barrier\n\t" \
  "v_add_u32 v116, s64, v110\n\t" \
  "v_add_u32 v117, s65, v111\n\t" \
  "ds_read_b128 v[64:67], v116\n\t" \
  "ds_read_b128 v[68:71], v116 offset:32\n\t" \
  "ds_read_b128 v[72:75], v117 offset:1024\n\t" \
  "ds_read_b128 v[76:79], v117\n\t" \
  "ds_read2_b32 v[114:115], v112 offset0:0 offset1:4\n\t" \
  "v_add_u32 v118, 66560, v111\n\t" \
  "v_mov_b32 v116, v110\n\t" \
  "v_add_u32 v125, 66560, v111\n\t" \
  "s_mov_b32 s44, 0\n\t" \
  "s_lshl_b32 s66, s47, 8\n\t" \
  "s_sub_u32 s66, 256, s66\n\t" \
  "v_add_u32 v112, s66, v112\n\t" \
  "s_waitcnt lgkmcnt(0)\n\t" \
  "v_mfma_f32_32x32x16_bf16 v[0:15], v[64:67], v[80:83], 0\n\t" \
  "v_mfma_f32_32x32x16_bf16 v[0:15], v[68:71], v[84:87], v[0:15]\n\t" \
  "v_mfma_f32_32x32x16_bf16 v[0:15], v[72:75], v[88:91], v[0:15]\n\t" \
  "v_mfma_f32_32x32x16_bf16 v[0:15], v[76:79], v[96:99], v[0:15]\n\t" \
  "s_cmp_eq_u32 s61, 2\n\t" \
  "s_cbranch_scc0 NE2_%=\n\t" \
  "s_add_u32 m0, s68, 1024\n\t" \
  "s_nop 0\n\t" \
  "global_load_lds_dwordx4 v20, s[54:55]\n\t" \
  "NE2_%=:\n\t" \
  "v_and_b32 v28, s70, v28\n\t" \
  "s_add_u32 m0, s74, s39\n\t" \
  "v_lshl_or_b32 v28, v28, 9, v113\n\t" \
  "global_load_lds_dwordx4 v28, s[52:53] sc1\n\t" \
  "v_and_b32 v29, s70, v29\n\t" \
  "s_add_u32 m0, m0, 1040\n\t" \
  "v_lshl_or_b32 v29, v29, 9, v113\n\t" \
  "global_load_lds_dwordx4 v29, s[52:53] sc1\n\t" \
  "s_cmp_eq_u32 s61, 3\n\t" \
  "s_cbranch_scc0 NE3_%=\n\t" \
  "s_add_u32 m0, s68, 1024\n\t" \
  "s_nop 0\n\t" \
  "global_load_lds_dwordx4 v20, s[54:55]\n\t" \
  "NE3_%=:\n\t" \
  "v_and_b32 v30, s70, v30\n\t" \
  "s_add_u32 m0, s75, s39\n\t" \
  "v_lshl_or_b32 v30, v30, 9, v113\n\t" \
  "global_load_lds_dwordx4 v30, s[52:53] sc1\n\t" \
  "v_and_b32 v31, s70, v31\n\t" \
  "s_add_u32 m0, m0, 1040\n\t" \
  "v_lshl_or_b32 v31, v31, 9, v113\n\t" \
  "global_load_lds_dwordx4 v31, s[52:53] sc1\n\t" \
  "s_cmp_ge_u32 s61, 4\n\t" \
  "s_cbranch_scc0 NE4_%=\n\t" \
  "s_add_u32 m0, s68, 1024\n\t" \
  "s_nop 0\n\t" \
  "global_load_lds_dwordx4 v20, s[54:55]\n\t" \
  "NE4_%=:\n\t" \
  "v_mov_b64 v[48:49], 0\n\t" \
  "v_mov_b64 v[50:51], 0\n\t" \
  "v_mov_b64 v[52:53], 0\n\t" \
  "v_mov_b64 v[54:55], 0\n\t" \
  "v_mov_b64 v[56:57], 0\n\t" \
  "v_mov_b64 v[58:59], 0\n\t" \
  "v_mov_b64 v[60:61], 0\n\t" \
  "v_mov_b64 v[62:63], 0\n\t" \
  "s_getpc_b64 s[70:71]\n\t" \
  "PCa_%=:\n\t" \
  "s_add_u32 s70, s70, RB_%=-PCa_%=\n\t" \
  "s_addc_u32 s71, s71, 0\n\t" \
  "s_getpc_b64 s[72:73]\n\t" \
  "PCb_%=:\n\t" \
  "s_add_u32 s72, s72, LOOP_%=-PCb_%=\n\t" \
  "s_addc_u32 s73, s73, 0\n\t" \
  "s_getpc_b64 s[74:75]\n\t" \
  "PCc_%=:\n\t" \
  "s_add_u32 s74, s74, TAIL_%=-PCc_%=\n\t" \
  "s_addc_u32 s75, s75, 0\n\t" \
  "s_cmp_eq_u32 s47, 0\n\t" \
  "s_cbranch_scc1 HEADA_%=\n\t" \
  "v_mov_b64 v[16:17], v[0:1]\n\t" \
  "v_mov_b64 v[18:19], v[2:3]\n\t" \
  "v_mov_b64 v[20:21], v[4:5]\n\t" \
  "v_mov_b64 v[22:23], v[6:7]\n\t" \
  "v_mov_b64 v[24:25], v[8:9]\n\t" \
  "v_mov_b64 v[26:27], v[10:11]\n\t" \
  "v_mov_b64 v[28:29], v[12:13]\n\t" \
  "v_mov_b64 v[30:31], v[14:15]\n\t" \
  "s_branch HEADB_%=\n\t" \
  "LOOP_%=:\n\t" \
  "v_exp_f32 v44, v12\n\t" \
  "v_exp_f32 v45, v13\n\t" \
  "v_exp_f32 v46, v14\n\t" \
  "v_exp_f32 v47, v15\n\t" \
  "v_exp_f32 v34, v2\n\t" \
  "v_pk_add_f32 v[46:47], v[46:47], 1.0 op_sel_hi:[1,0]\n\t" \
  "v_exp_f32 v32, v0\n\t" \
  "s_mov_b32 m0, s38\n\t" \
  "ds_read2_b32 v[114:115], v112 offset0:0 offset1:4\n\t" \
  "global_load_lds_dwordx4 v119, s[52:53] sc1\n\t" \
  "s_add_u32 m0, s38, 1040\n\t" \
  "v_pk_add_f32 v[44:45], v[44:45], 1.0 op_sel_hi:[1,0]\n\t" \
  "s_sub_u32 s38, s49, s38\n\t" \
  "v_exp_f32 v36, v4\n\t" \
  "global_load_lds_dwordx4 v124, s[52:53] sc1\n\t" \
  "v_pk_add_f32 v[40:41], v[40:41], 1.0 op_sel_hi:[1,0]\n\t" \
  "v_exp_f32 v38, v6\n\t" \
  "v_exp_f32 v37, v5\n\t" \
  "v_exp_f32 v35, v3\n\t" \
  "v_exp_f32 v33, v1\n\t" \
  "v_exp_f32 v39, v7\n\t" \
  "v_pk_add_f32 v[32:33], v[32:33], 1.0 op_sel_hi:[1,0]\n\t" \
  "v_rcp_f32 v45, v45\n\t" \
  "v_pk_add_f32 v[42:43], v[42:43], 1.0 op_sel_hi:[1,0]\n\t" \
  "v_pk_add_f32 v[38:39], v[38:39], 1.0 op_sel_hi:[1,0]\n\t" \
  "v_pk_add_f32 v[34:35], v[34:35], 1.0 op_sel_hi:[1,0]\n\t" \
  "v_pk_add_f32 v[36:37], v[36:37], 1.0 op_sel_hi:[1,0]\n\t" \
  "s_waitcnt lgkmcnt(1)\n\t" \
  "v_mfma_f32_32x32x16_bf16 v[16:31], v[64:67], v[80:83], 0\n\t" \
  "v_rcp_f32 v43, v43\n\t" \
  "v_rcp_f32 v33, v33\n\t" \
  "v_rcp_f32 v37, v37\n\t" \
  "v_mfma_f32_32x32x16_bf16 v[16:31], v[68:71], v[84:87], v[16:31]\n\t" \
  "ds_read_b128 v[64:67], v118 offset:2048\n\t" \
  "ds_read_b128 v[68:71], v118 offset:3072\n\t" \
  "v_rcp_f32 v35, v35\n\t" \
  "v_rcp_f32 v38, v38\n\t" \
  "v_mfma_f32_32x32x16_bf16 v[16:31], v[72:75], v[88:91], v[16:31]\n\t" \
  "v_rcp_f32 v39, v39\n\t" \
  "v_rcp_f32 v36, v36\n\t" \
  "v_rcp_f32 v41, v41\n\t" \
  "v_rcp_f32 v34, v34\n\t" \
  "v_mfma_f32_32x32x16_bf16 v[16:31], v[76:79], v[96:99], v[16:31]\n\t" \
  "v_rcp_f32 v32, v32\n\t" \
  "v_rcp_f32 v40, v40\n\t" \
  "v_rcp_f32 v47, v47\n\t" \
  "v_rcp_f32 v46, v46\n\t" \
  "v_rcp_f32 v42, v42\n\t" \
  "v_pk_mul_f32 v[32:33], v[32:33], v[0:1]\n\t" \
  "v_pk_mul_f32 v[34:35], v[34:35], v[2:3]\n\t" \
  "v_pk_mul_f32 v[40:41], v[40:41], v[8:9]\n\t" \
  "v_pk_mul_f32 v[42:43], v[42:43], v[10:11]\n\t" \
  "v_pk_mul_f32 v[38:39], v[38:39], v[6:7]\n\t" \
  "v_cvt_pk_bf16_f32 v32, v32, v33\n\t" \
  "v_rcp_f32 v44, v44\n\t" \
  "v_pk_mul_f32 v[36:37], v[36:37], v[4:5]\n\t" \
  "v_cvt_pk_bf16_f32 v33, v34, v35\n\t" \
  "v_cvt_pk_bf16_f32 v34, v36, v37\n\t" \
  "v_cvt_pk_bf16_f32 v35, v38, v39\n\t" \
  "v_pk_mul_f32 v[46:47], v[46:47], v[14:15]\n\t" \
  "v_pk_mul_f32 v[44:45], v[44:45], v[12:13]\n\t" \
  "s_waitcnt lgkmcnt(1)\n\t" \
  "v_mfma_f32_32x32x16_bf16 v[48:63], v[32:35], v[64:67], v[48:63]\n\t" \
  "v_cvt_pk_bf16_f32 v38, v44, v45\n\t" \
  "v_cvt_pk_bf16_f32 v36, v40, v41\n\t" \
  "v_cvt_pk_bf16_f32 v37, v42, v43\n\t" \
  "v_cvt_pk_bf16_f32 v39, v46, v47\n\t" \
  "s_add_u32 s36, s36, 1\n\t" \
  "s_waitcnt lgkmcnt(0)\n\t" \
  "v_mfma_f32_32x32x16_bf16 v[48:63], v[36:39], v[68:71], v[48:63]\n\t" \
  "HEADB_%=:\n\t" \
  "s_waitcnt vmcnt(4)\n\t" \
  "s_barrier\n\t" \
  "ds_read_b128 v[72:75], v118 offset:5120\n\t" \
  "v_exp_f32 v41, v25\n\t" \
  "ds_read_b128 v[68:71], v116 offset:16672\n\t" \
  "v_or_b32 v124, v115, v113\n\t" \
  "v_exp_f32 v40, v24\n\t" \
  "v_exp_f32 v42, v26\n\t" \
  "v_or_b32 v119, v114, v113\n\t" \
  "v_exp_f32 v43, v27\n\t" \
  "ds_read_b128 v[64:67], v116 offset:16640\n\t" \
  "s_bitcmp1_b32 s41, s36\n\t" \
  "ds_read_b128 v[76:79], v118 offset:4096\n\t" \
  "s_cbranch_scc1 EV1_%=\n\t" \
  "RB_%=:\n\t" \
  "v_exp_f32 v44, v28\n\t" \
  "v_exp_f32 v45, v29\n\t" \
  "v_exp_f32 v46, v30\n\t" \
  "v_exp_f32 v47, v31\n\t" \
  "v_exp_f32 v34, v18\n\t" \
  "v_pk_add_f32 v[46:47], v[46:47], 1.0 op_sel_hi:[1,0]\n\t" \
  "v_exp_f32 v32, v16\n\t" \
  "s_mov_b32 m0, s39\n\t" \
  "s_sub_u32 s48, 33280, s48\n\t" \
  "ds_read2_b32 v[114:115], v112 offset0:64 offset1:68\n\t" \
  "global_load_lds_dwordx4 v119, s[52:53] sc1\n\t" \
  "s_add_u32 m0, s39, 1040\n\t" \
  "v_add_u32 v116, s48, v110\n\t" \
  "v_pk_add_f32 v[44:45], v[44:45], 1.0 op_sel_hi:[1,0]\n\t" \
  "s_sub_u32 s39, s50, s39\n\t" \
  "v_exp_f32 v36, v20\n\t" \
  "s_add_u32 s44, s44, 8192\n\t" \
  "global_load_lds_dwordx4 v124, s[52:53] sc1\n\t" \
  "v_pk_add_f32 v[40:41], v[40:41], 1.0 op_sel_hi:[1,0]\n\t" \
  "v_exp_f32 v38, v22\n\t" \
  "v_exp_f32 v37, v21\n\t" \
  "v_add_u32 v112, 512, v112\n\t" \
  "v_exp_f32 v35, v19\n\t" \
  "v_exp_f32 v33, v17\n\t" \
  "v_exp_f32 v39, v23\n\t" \
  "v_pk_add_f32 v[32:33], v[32:33], 1.0 op_sel_hi:[1,0]\n\t" \
  "v_rcp_f32 v45, v45\n\t" \
  "v_pk_add_f32 v[42:43], v[42:43], 1.0 op_sel_hi:[1,0]\n\t" \
  "v_pk_add_f32 v[38:39], v[38:39], 1.0 op_sel_hi:[1,0]\n\t" \
  "v_pk_add_f32 v[34:35], v[34:35], 1.0 op_sel_hi:[1,0]\n\t" \
  "v_pk_add_f32 v[36:37], v[36:37], 1.0 op_sel_hi:[1,0]\n\t" \
  "s_waitcnt lgkmcnt(1)\n\t" \
  "v_mfma_f32_32x32x16_bf16 v[0:15], v[64:67], v[80:83], 0\n\t" \
  "v_rcp_f32 v43, v43\n\t" \
  "v_rcp_f32 v33, v33\n\t" \
  "v_rcp_f32 v37, v37\n\t" \
  "v_mfma_f32_32x32x16_bf16 v[0:15], v[68:71], v[84:87], v[0:15]\n\t" \
  "ds_read_b128 v[64:67], v118 offset:6144\n\t" \
  "ds_read_b128 v[68:71], v118 offset:7168\n\t" \
  "v_rcp_f32 v35, v35\n\t" \
  "v_rcp_f32 v38, v38\n\t" \
  "v_mfma_f32_32x32x16_bf16 v[0:15], v[72:75], v[88:91], v[0:15]\n\t" \
  "s_and_b32 s44, s44, 0x7fff\n\t" \
  "v_rcp_f32 v39, v39\n\t" \
  "v_rcp_f32 v36, v36\n\t" \
  "v_rcp_f32 v41, v41\n\t" \
  "v_rcp_f32 v34, v34\n\t" \
  "v_mfma_f32_32x32x16_bf16 v[0:15], v[76:79], v[96:99], v[0:15]\n\t" \
  "v_rcp_f32 v32, v32\n\t" \
  "v_rcp_f32 v40, v40\n\t" \
  "v_rcp_f32 v47, v47\n\t" \
  "v_rcp_f32 v46, v46\n\t" \
  "v_rcp_f32 v42, v42\n\t" \
  "v_pk_mul_f32 v[32:33], v[32:33], v[16:17]\n\t" \
  "v_pk_mul_f32 v[34:35], v[34:35], v[18:19]\n\t" \
  "v_pk_mul_f32 v[40:41], v[40:41], v[24:25]\n\t" \
  "v_pk_mul_f32 v[42:43], v[42:43], v[26:27]\n\t" \
  "v_pk_mul_f32 v[38:39], v[38:39], v[22:23]\n\t" \
  "v_cvt_pk_bf16_f32 v32, v32, v33\n\t" \
  "v_rcp_f32 v44, v44\n\t" \
  "v_pk_mul_f32 v[36:37], v[36:37], v[20:21]\n\t" \
  "v_add_u32 v118, s44, v125\n\t" \
  "v_cvt_pk_bf16_f32 v33, v34, v35\n\t" \
  "v_cvt_pk_bf16_f32 v34, v36, v37\n\t" \
  "v_cvt_pk_bf16_f32 v35, v38, v39\n\t" \
  "v_pk_mul_f32 v[46:47], v[46:47], v[30:31]\n\t" \
  "v_pk_mul_f32 v[44:45], v[44:45], v[28:29]\n\t" \
  "s_waitcnt lgkmcnt(1)\n\t" \
  "v_mfma_f32_32x32x16_bf16 v[48:63], v[32:35], v[64:67], v[48:63]\n\t" \
  "v_cvt_pk_bf16_f32 v38, v44, v45\n\t" \
  "v_cvt_pk_bf16_f32 v36, v40, v41\n\t" \
  "v_cvt_pk_bf16_f32 v37, v42, v43\n\t" \
  "v_cvt_pk_bf16_f32 v39, v46, v47\n\t" \
  "s_add_u32 s36, s36, 1\n\t" \
  "s_waitcnt lgkmcnt(0)\n\t" \
  "v_mfma_f32_32x32x16_bf16 v[48:63], v[36:39], v[68:71], v[48:63]\n\t" \
  "HEADA_%=:\n\t" \
  "s_waitcnt vmcnt(4)\n\t" \
  "s_barrier\n\t" \
  "ds_read_b128 v[72:75], v118 offset:1024\n\t" \
  "v_exp_f32 v41, v9\n\t" \
  "ds_read_b128 v[68:71], v116 offset:32\n\t" \
  "v_or_b32 v124, v115, v113\n\t" \
  "v_exp_f32 v40, v8\n\t" \
  "v_exp_f32 v42, v10\n\t" \
  "v_or_b32 v119, v114, v113\n\t" \
  "v_exp_f32 v43, v11\n\t" \
  "ds_read_b128 v[64:67], v116 offset:0\n\t" \
  "s_bitcmp1_b32 s41, s36\n\t" \
  "ds_read_b128 v[76:79], v118 offset:0\n\t" \
  "s_cbranch_scc0 LOOP_%=\n\t" \
  "s_cmp_lt_u32 s36, s37\n\t" \
  "s_cselect_b64 s[68:69], s[72:73], s[74:75]\n\t" \
  "EV0_%=:\n\t" \
  "s_bitcmp1_b32 s40, s36\n\t" \
  "s_cbranch_scc1 PR0_%=\n\t" \
  "s_bitcmp1_b32 s42, s36\n\t" \
  "s_cbranch_scc0 NF0_%=\n\t" \
  "FL0_%=:\n\t" \
  "s_add_u32 s76, s68, 16\n\t" \
  "s_addc_u32 s77, s69, 0\n\t" \
  "ds_read_b128 v[24:27], v122\n\t" \
  "ds_read_b128 v[28:31], v122 offset:1024\n\t" \
  "v_cvt_pk_bf16_f32 v16, v48, v49\n\t" \
  "v_cvt_pk_bf16_f32 v17, v50, v51\n\t" \
  "v_cvt_pk_bf16_f32 v18, v52, v53\n\t" \
  "v_cvt_pk_bf16_f32 v19, v54, v55\n\t" \
  "v_cvt_pk_bf16_f32 v20, v56, v57\n\t" \
  "v_cvt_pk_bf16_f32 v21, v58, v59\n\t" \
  "v_cvt_pk_bf16_f32 v22, v60, v61\n\t" \
  "v_cvt_pk_bf16_f32 v23, v62, v63\n\t" \
  "ds_write2_b64 v120, v[16:17], v[18:19] offset0:0 offset1:2\n\t" \
  "ds_write2_b64 v120, v[20:21], v[22:23] offset0:4 offset1:6\n\t" \
  "v_pk_mul_f32 v[48:49], v[48:49], v[128:129]\n\t" \
  "v_pk_mul_f32 v[50:51], v[50:51], v[130:131]\n\t" \
  "v_pk_mul_f32 v[52:53], v[52:53], v[132:133]\n\t" \
  "v_pk_mul_f32 v[54:55], v[54:55], v[134:135]\n\t" \
  "v_add_u32 v120, 3696, v120\n\t" \
  "v_add_u32 v122, 2048, v122\n\t" \
  "v_pk_fma_f32 v[16:17], v[56:57], v[136:137], v[48:49]\n\t" \
  "v_pk_fma_f32 v[18:19], v[58:59], v[138:139], v[50:51]\n\t" \
  "v_pk_fma_f32 v[20:21], v[60:61], v[140:141], v[52:53]\n\t" \
  "v_pk_fma_f32 v[22:23], v[62:63], v[142:143], v[54:55]\n\t" \
  "s_waitcnt lgkmcnt(2)\n\t" \
  "v_mfma_f32_32x32x16_bf16 v[48:63], v[32:35], v[24:27], 0\n\t" \
  "v_mov_b64 v[96:97], v[98:99]\n\t" \
  "v_mov_b64 v[98:99], v[100:101]\n\t" \
  "v_pk_add_f32 v[16:17], v[16:17], v[18:19]\n\t" \
  "v_pk_add_f32 v[20:21], v[20:21], v[22:23]\n\t" \
  "v_mov_b64 v[100:101], v[102:103]\n\t" \
  "v_mov_b64 v[102:103], v[104:105]\n\t" \
  "v_exp_f32 v44, v12\n\t" \
  "v_exp_f32 v45, v13\n\t" \
  "v_pk_add_f32 v[16:17], v[16:17], v[20:21]\n\t" \
  "v_mfma_f32_32x32x16_bf16 v[48:63], v[36:39], v[28:31], v[48:63]\n\t" \
  "v_mov_b64 v[104:105], v[106:107]\n\t" \
  "v_mov_b64 v[106:107], v[108:109]\n\t" \
  "v_exp_f32 v46, v14\n\t" \
  "v_exp_f32 v47, v15\n\t" \
  "v_add_f32 v16, v16, v17\n\t" \
  "ds_write_b32 v121, v16\n\t" \
  "v_add_u32 v121, 2048, v121\n\t" \
  "s_setpc_b64 s[76:77]\n\t" \
  "PR0_%=:\n\t" \
  "s_bitcmp1_b32 s42, s36\n\t" \
  "s_cbranch_scc1 PF0_%=\n\t" \
  "s_add_u32 s76, s68, 16\n\t" \
  "s_addc_u32 s77, s69, 0\n\t" \
  "ds_read_b128 v[16:19], v123\n\t" \
  "v_mov_b64 v[20:21], 0\n\t" \
  "v_mov_b64 v[22:23], 0\n\t" \
  "v_add_u32 v25, s45, v111\n\t" \
  "v_add_u32 v26, s46, v111\n\t" \
  "ds_write_b128 v25, v[20:23] offset:2048\n\t" \
  "ds_write_b128 v25, v[20:23] offset:3072\n\t" \
  "ds_write_b128 v26, v[20:23]\n\t" \
  "s_add_u32 m0, s46, 1024\n\t" \
  "v_add_u32 v123, 8192, v123\n\t" \
  "v_exp_f32 v44, v12\n\t" \
  "v_exp_f32 v45, v13\n\t" \
  "v_exp_f32 v46, v14\n\t" \
  "v_exp_f32 v47, v15\n\t" \
  "s_waitcnt lgkmcnt(3)\n\t" \
  "v_add_u32_sdwa v25, s45, v18 dst_sel:DWORD dst_unused:UNUSED_PAD src0_sel:DWORD src1_sel:WORD_0\n\t" \
  "v_add_u32_sdwa v26, s46, v18 dst_sel:DWORD dst_unused:UNUSED_PAD src0_sel:DWORD src1_sel:WORD_1\n\t" \
  "global_load_lds_dwordx4 v19, s[54:55]\n\t" \
  "ds_write_b16 v25, v16 offset:2048\n\t" \
  "ds_write_b16_d16_hi v25, v16 offset:2064\n\t" \
  "ds_write_b16 v26, v17\n\t" \
  "ds_write_b16_d16_hi v26, v17 offset:8\n\t" \
  "s_setpc_b64 s[76:77]\n\t" \
  "PF0_%=:\n\t" \
  "ds_read_b128 v[16:19], v123\n\t" \
  "v_mov_b64 v[20:21], 0\n\t" \
  "v_mov_b64 v[22:23], 0\n\t" \
  "v_add_u32 v25, s45, v111\n\t" \
  "v_add_u32 v26, s46, v111\n\t" \
  "ds_write_b128 v25, v[20:23] offset:2048\n\t" \
  "ds_write_b128 v25, v[20:23] offset:3072\n\t" \
  "ds_write_b128 v26, v[20:23]\n\t" \
  "s_add_u32 m0, s46, 1024\n\t" \
  "v_add_u32 v123, 8192, v123\n\t" \
  "s_waitcnt lgkmcnt(3)\n\t" \
  "v_add_u32_sdwa v25, s45, v18 dst_sel:DWORD dst_unused:UNUSED_PAD src0_sel:DWORD src1_sel:WORD_0\n\t" \
  "v_add_u32_sdwa v26, s46, v18 dst_sel:DWORD dst_unused:UNUSED_PAD src0_sel:DWORD src1_sel:WORD_1\n\t" \
  "global_load_lds_dwordx4 v19, s[54:55]\n\t" \
  "ds_write_b16 v25, v16 offset:2048\n\t" \
  "ds_write_b16_d16_hi v25, v16 offset:2064\n\t" \
  "ds_write_b16 v26, v17\n\t" \
  "ds_write_b16_d16_hi v26, v17 offset:8\n\t" \
  "s_waitcnt lgkmcnt(0)\n\t" \
  "s_branch FL0_%=\n\t" \
  "NF0_%=:\n\t" \
  "s_setpc_b64 s[68:69]\n\t" \
  "EV1_%=:\n\t" \
  "s_bitcmp1_b32 s40, s36\n\t" \
  "s_cbranch_scc1 PR1_%=\n\t" \
  "s_bitcmp1_b32 s42, s36\n\t" \
  "s_cbranch_scc0 NF1_%=\n\t" \
  "FL1_%=:\n\t" \
  "s_add_u32 s76, s70, 16\n\t" \
  "s_addc_u32 s77, s71, 0\n\t" \
  "ds_read_b128 v[8:11], v122\n\t" \
  "ds_read_b128 v[12:15], v122 offset:1024\n\t" \
  "v_cvt_pk_bf16_f32 v0, v48, v49\n\t" \
  "v_cvt_pk_bf16_f32 v1, v50, v51\n\t" \
  "v_cvt_pk_bf16_f32 v2, v52, v53\n\t" \
  "v_cvt_pk_bf16_f32 v3, v54, v55\n\t" \
  "v_cvt_pk_bf16_f32 v4, v56, v57\n\t" \
  "v_cvt_pk_bf16_f32 v5, v58, v59\n\t" \
  "v_cvt_pk_bf16_f32 v6, v60, v61\n\t" \
  "v_cvt_pk_bf16_f32 v7, v62, v63\n\t" \
  "ds_write2_b64 v120, v[0:1], v[2:3] offset0:0 offset1:2\n\t" \
  "ds_write2_b64 v120, v[4:5], v[6:7] offset0:4 offset1:6\n\t" \
  "v_pk_mul_f32 v[48:49], v[48:49], v[128:129]\n\t" \
  "v_pk_mul_f32 v[50:51], v[50:51], v[130:131]\n\t" \
  "v_pk_mul_f32 v[52:53], v[52:53], v[132:133]\n\t" \
  "v_pk_mul_f32 v[54:55], v[54:55], v[134:135]\n\t" \
  "v_add_u32 v120, 3696, v120\n\t" \
  "v_add_u32 v122, 2048, v122\n\t" \
  "v_pk_fma_f32 v[0:1], v[56:57], v[136:137], v[48:49]\n\t" \
  "v_pk_fma_f32 v[2:3], v[58:59], v[138:139], v[50:51]\n\t" \
  "v_pk_fma_f32 v[4:5], v[60:61], v[140:141], v[52:53]\n\t" \
  "v_pk_fma_f32 v[6:7], v[62:63], v[142:143], v[54:55]\n\t" \
  "s_waitcnt lgkmcnt(2)\n\t" \
  "v_mfma_f32_32x32x16_bf16 v[48:63], v[32:35], v[8:11], 0\n\t" \
  "v_mov_b64 v[96:97], v[98:99]\n\t" \
  "v_mov_b64 v[98:99], v[100:101]\n\t" \
  "v_pk_add_f32 v[0:1], v[0:1], v[2:3]\n\t" \
  "v_pk_add_f32 v[4:5], v[4:5], v[6:7]\n\t" \
  "v_mov_b64 v[100:101], v[102:103]\n\t" \
  "v_mov_b64 v[102:103], v[104:105]\n\t" \
  "v_exp_f32 v44, v28\n\t" \
  "v_exp_f32 v45, v29\n\t" \
  "v_pk_add_f32 v[0:1], v[0:1], v[4:5]\n\t" \
  "v_mfma_f32_32x32x16_bf16 v[48:63], v[36:39], v[12:15], v[48:63]\n\t" \
  "v_mov_b64 v[104:105], v[106:107]\n\t" \
  "v_mov_b64 v[106:107], v[108:109]\n\t" \
  "v_exp_f32 v46, v30\n\t" \
  "v_exp_f32 v47, v31\n\t" \
  "v_add_f32 v0, v0, v1\n\t" \
  "ds_write_b32 v121, v0\n\t" \
  "v_add_u32 v121, 2048, v121\n\t" \
  "s_setpc_b64 s[76:77]\n\t" \
  "PR1_%=:\n\t" \
  "s_bitcmp1_b32 s42, s36\n\t" \
  "s_cbranch_scc1 PF1_%=\n\t" \
  "s_add_u32 s76, s70, 16\n\t" \
  "s_addc_u32 s77, s71, 0\n\t" \
  "ds_read_b128 v[0:3], v123\n\t" \
  "v_mov_b64 v[4:5], 0\n\t" \
  "v_mov_b64 v[6:7], 0\n\t" \
  "v_add_u32 v9, s45, v111\n\t" \
  "v_add_u32 v10, s46, v111\n\t" \
  "ds_write_b128 v9, v[4:7] offset:2048\n\t" \
  "ds_write_b128 v9, v[4:7] offset:3072\n\t" \
  "ds_write_b128 v10, v[4:7]\n\t" \
  "s_add_u32 m0, s46, 1024\n\t" \
  "v_add_u32 v123, 8192, v123\n\t" \
  "v_exp_f32 v44, v28\n\t" \
  "v_exp_f32 v45, v29\n\t" \
  "v_exp_f32 v46, v30\n\t" \
  "v_exp_f32 v47, v31\n\t" \
  "s_waitcnt lgkmcnt(3)\n\t" \
  "v_add_u32_sdwa v9, s45, v2 dst_sel:DWORD dst_unused:UNUSED_PAD src0_sel:DWORD src1_sel:WORD_0\n\t" \
  "v_add_u32_sdwa v10, s46, v2 dst_sel:DWORD dst_unused:UNUSED_PAD src0_sel:DWORD src1_sel:WORD_1\n\t" \
  "global_load_lds_dwordx4 v3, s[54:55]\n\t" \
  "ds_write_b16 v9, v0 offset:2048\n\t" \
  "ds_write_b16_d16_hi v9, v0 offset:2064\n\t" \
  "ds_write_b16 v10, v1\n\t" \
  "ds_write_b16_d16_hi v10, v1 offset:8\n\t" \
  "s_setpc_b64 s[76:77]\n\t" \
  "PF1_%=:\n\t" \
  "ds_read_b128 v[0:3], v123\n\t" \
  "v_mov_b64 v[4:5], 0\n\t" \
  "v_mov_b64 v[6:7], 0\n\t" \
  "v_add_u32 v9, s45, v111\n\t" \
  "v_add_u32 v10, s46, v111\n\t" \
  "ds_write_b128 v9, v[4:7] offset:2048\n\t" \
  "ds_write_b128 v9, v[4:7] offset:3072\n\t" \
  "ds_write_b128 v10, v[4:7]\n\t" \
  "s_add_u32 m0, s46, 1024\n\t" \
  "v_add_u32 v123, 8192, v123\n\t" \
  "s_waitcnt lgkmcnt(3)\n\t" \
  "v_add_u32_sdwa v9, s45, v2 dst_sel:DWORD dst_unused:UNUSED_PAD src0_sel:DWORD src1_sel:WORD_0\n\t" \
  "v_add_u32_sdwa v10, s46, v2 dst_sel:DWORD dst_unused:UNUSED_PAD src0_sel:DWORD src1_sel:WORD_1\n\t" \
  "global_load_lds_dwordx4 v3, s[54:55]\n\t" \
  "ds_write_b16 v9, v0 offset:2048\n\t" \
  "ds_write_b16_d16_hi v9, v0 offset:2064\n\t" \
  "ds_write_b16 v10, v1\n\t" \
  "ds_write_b16_d16_hi v10, v1 offset:8\n\t" \
  "s_waitcnt lgkmcnt(0)\n\t" \
  "s_branch FL1_%=\n\t" \
  "NF1_%=:\n\t" \
  "s_setpc_b64 s[70:71]\n\t" \
  "MORE_%=:\n\t" \
  "v_lshrrev_b32 v12, 8, v145\n\t" \
  "v_lshlrev_b32 v12, 8, v12\n\t" \
  "v_mul_u32_u24 v13, 43, v149\n\t" \
  "v_lshrrev_b32 v13, 7, v13\n\t" \
  "v_add_u32 v13, 36, v13\n\t" \
  "v_min_u32 v14, 47, v13\n\t" \
  "v_lshl_add_u32 v12, v14, 2, v12\n\t" \
  "global_load_dword v14, v12, s[82:83] sc1\n\t" \
  "s_waitcnt vmcnt(0)\n\t" \
  "v_lshrrev_b32 v12, 28, v14\n\t" \
  "v_and_or_b32 v12, v12, 14, v147\n\t" \
  "ds_read_u16 v12, v12\n\t" \
  "v_cmp_lt_u32 vcc, v13, v7\n\t" \
  "s_waitcnt lgkmcnt(0)\n\t" \
  "v_add_u16 v12, v12, v13\n\t" \
  "v_lshl_add_u32 v12, v12, 3, s67\n\t" \
  "v_cndmask_b32 v12, v22, v12, vcc\n\t" \
  "ds_write2_b32 v12, v14, v148 offset1:1\n\t" \
  "s_branch BACK_%=\n\t" \
  "TAIL_%=:\n\t" \
  "v_exp_f32 v44, v12\n\t" \
  "v_exp_f32 v45, v13\n\t" \
  "v_exp_f32 v46, v14\n\t" \
  "v_exp_f32 v47, v15\n\t" \
  "v_exp_f32 v34, v2\n\t" \
  "v_pk_add_f32 v[46:47], v[46:47], 1.0 op_sel_hi:[1,0]\n\t" \
  "v_lshrrev_b32 v117, 9, v111\n\t" \
  "s_lshl_b32 s66, s61, 1\n\t" \
  "v_add_u32 v117, s66, v117\n\t" \
  "v_and_b32 v126, 15, v117\n\t" \
  "v_lshlrev_b32 v126, 4, v126\n\t" \
  "v_and_b32 v127, 0x1f0, v111\n\t" \
  "v_xor_b32 v126, v126, v127\n\t" \
  "v_lshl_add_u32 v117, v117, 8, s60\n\t" \
  "s_lshl_b32 s66, s61, 10\n\t" \
  "v_exp_f32 v32, v0\n\t" \
  "s_cmp_gt_u32 s61, 3\n\t" \
  "s_cbranch_scc1 NOH_%=\n\t" \
  "v_add_u32 v127, 8192, v117\n\t" \
  "v_min_u32 v127, 9999, v127\n\t" \
  "s_add_u32 m0, s66, 150528\n\t" \
  "v_lshl_add_u32 v127, v127, 9, v126\n\t" \
  "global_load_lds_dwordx4 v127, s[56:57]\n\t" \
  "NOH_%=:\n\t" \
  "v_pk_add_f32 v[44:45], v[44:45], 1.0 op_sel_hi:[1,0]\n\t" \
  "v_exp_f32 v36, v4\n\t" \
  "v_add_u32 v127, 0, v117\n\t" \
  "v_min_u32 v127, 9999, v127\n\t" \
  "s_add_u32 m0, s66, 134144\n\t" \
  "v_lshl_add_u32 v127, v127, 9, v126\n\t" \
  "global_load_lds_dwordx4 v127, s[56:57]\n\t" \
  "v_pk_add_f32 v[40:41], v[40:41], 1.0 op_sel_hi:[1,0]\n\t" \
  "v_exp_f32 v38, v6\n\t" \
  "v_add_u32 v127, 4096, v117\n\t" \
  "v_min_u32 v127, 9999, v127\n\t" \
  "s_add_u32 m0, s66, 142336\n\t" \
  "v_lshl_add_u32 v127, v127, 9, v126\n\t" \
  "global_load_lds_dwordx4 v127, s[56:57]\n\t" \
  "v_exp_f32 v37, v5\n\t" \
  "v_exp_f32 v35, v3\n\t" \
  "v_bfe_u32 v117, v111, 4, 5\n\t" \
  "v_lshrrev_b32 v126, 9, v111\n\t" \
  "v_and_b32 v127, 15, v117\n\t" \
  "v_xor_b32 v127, v127, v126\n\t" \
  "v_lshlrev_b32 v127, 4, v127\n\t" \
  "v_lshl_add_u32 v112, v117, 9, v127\n\t" \
  "v_add_u32 v112, 134144, v112\n\t" \
  "v_exp_f32 v33, v1\n\t" \
  "v_lshlrev_b32 v126, 4, v126\n\t" \
  "v_mul_u32_u24 v114, 528, v117\n\t" \
  "v_lshl_add_u32 v113, v117, 17, v126\n\t" \
  "v_add_u32 v126, 111616, v126\n\t" \
  "v_add_u32 v114, v114, v126\n\t" \
  "v_exp_f32 v39, v7\n\t" \
  "v_pk_add_f32 v[32:33], v[32:33], 1.0 op_sel_hi:[1,0]\n\t" \
  "v_rcp_f32 v45, v45\n\t" \
  "s_add_u32 s64, s58, 0\n\t" \
  "s_addc_u32 s65, s59, 0\n\t" \
  "global_load_dwordx4 v[144:147], v111, s[64:65] sc1\n\t" \
  "v_pk_add_f32 v[42:43], v[42:43], 1.0 op_sel_hi:[1,0]\n\t" \
  "v_pk_add_f32 v[38:39], v[38:39], 1.0 op_sel_hi:[1,0]\n\t" \
  "v_pk_add_f32 v[34:35], v[34:35], 1.0 op_sel_hi:[1,0]\n\t" \
  "v_pk_add_f32 v[36:37], v[36:37], 1.0 op_sel_hi:[1,0]\n\t" \
  "s_waitcnt lgkmcnt(0)\n\t" \
  "v_mfma_f32_32x32x16_bf16 v[16:31], v[64:67], v[80:83], 0\n\t" \
  "v_rcp_f32 v43, v43\n\t" \
  "s_add_u32 s64, s58, 1024\n\t" \
  "s_addc_u32 s65, s59, 0\n\t" \
  "global_load_dwordx4 v[148:151], v111, s[64:65] sc1\n\t" \
  "v_rcp_f32 v33, v33\n\t" \
  "v_rcp_f32 v37, v37\n\t" \
  "v_mfma_f32_32x32x16_bf16 v[16:31], v[68:71], v[84:87], v[16:31]\n\t" \
  "ds_read_b128 v[64:67], v118 offset:2048\n\t" \
  "ds_read_b128 v[68:71], v118 offset:3072\n\t" \
  "s_add_u32 s64, s58, 2048\n\t" \
  "s_addc_u32 s65, s59, 0\n\t" \
  "global_load_dwordx4 v[152:155], v111, s[64:65] sc1\n\t" \
  "v_rcp_f32 v35, v35\n\t" \
  "v_rcp_f32 v38, v38\n\t" \
  "v_mfma_f32_32x32x16_bf16 v[16:31], v[72:75], v[88:91], v[16:31]\n\t" \
  "v_rcp_f32 v39, v39\n\t" \
  "v_rcp_f32 v36, v36\n\t" \
  "v_rcp_f32 v41, v41\n\t" \
  "s_add_u32 s64, s58, 3072\n\t" \
  "s_addc_u32 s65, s59, 0\n\t" \
  "global_load_dwordx4 v[156:159], v111, s[64:65] sc1\n\t" \
  "v_rcp_f32 v34, v34\n\t" \
  "v_mfma_f32_32x32x16_bf16 v[16:31], v[76:79], v[96:99], v[16:31]\n\t" \
  "v_rcp_f32 v32, v32\n\t" \
  "v_rcp_f32 v40, v40\n\t" \
  "v_rcp_f32 v47, v47\n\t" \
  "v_rcp_f32 v46, v46\n\t" \
  "s_add_u32 s64, s58, 4096\n\t" \
  "s_addc_u32 s65, s59, 0\n\t" \
  "global_load_dwordx4 v[160:163], v111, s[64:65] sc1\n\t" \
  "v_rcp_f32 v42, v42\n\t" \
  "v_pk_mul_f32 v[32:33], v[32:33], v[0:1]\n\t" \
  "v_pk_mul_f32 v[34:35], v[34:35], v[2:3]\n\t" \
  "v_pk_mul_f32 v[40:41], v[40:41], v[8:9]\n\t" \
  "v_pk_mul_f32 v[42:43], v[42:43], v[10:11]\n\t" \
  "s_add_u32 s64, s58, 5120\n\t" \
  "s_addc_u32 s65, s59, 0\n\t" \
  "global_load_dwordx4 v[164:167], v111, s[64:65] sc1\n\t" \
  "v_pk_mul_f32 v[38:39], v[38:39], v[6:7]\n\t" \
  "v_cvt_pk_bf16_f32 v32, v32, v33\n\t" \
  "v_rcp_f32 v44, v44\n\t" \
  "v_pk_mul_f32 v[36:37], v[36:37], v[4:5]\n\t" \
  "v_cvt_pk_bf16_f32 v33, v34, v35\n\t" \
  "v_cvt_pk_bf16_f32 v34, v36, v37\n\t" \
  "s_add_u32 s64, s58, 6144\n\t" \
  "s_addc_u32 s65, s59, 0\n\t" \
  "global_load_dwordx4 v[168:171], v111, s[64:65] sc1\n\t" \
  "v_cvt_pk_bf16_f32 v35, v38, v39\n\t" \
  "v_pk_mul_f32 v[46:47], v[46:47], v[14:15]\n\t" \
  "v_pk_mul_f32 v[44:45], v[44:45], v[12:13]\n\t" \
  "s_waitcnt lgkmcnt(1)\n\t" \
  "v_mfma_f32_32x32x16_bf16 v[48:63], v[32:35], v[64:67], v[48:63]\n\t" \
  "v_cvt_pk_bf16_f32 v38, v44, v45\n\t" \
  "v_cvt_pk_bf16_f32 v36, v40, v41\n\t" \
  "v_cvt_pk_bf16_f32 v37, v42, v43\n\t" \
  "v_cvt_pk_bf16_f32 v39, v46, v47\n\t" \
  "s_add_u32 s36, s36, 1\n\t" \
  "s_waitcnt lgkmcnt(0)\n\t" \
  "v_mfma_f32_32x32x16_bf16 v[48:63], v[36:39], v[68:71], v[48:63]\n\t" \
  "s_getpc_b64 s[70:71]\n\t" \
  "PCd_%=:\n\t" \
  "s_add_u32 s70, s70, R1_%=-PCd_%=\n\t" \
  "s_addc_u32 s71, s71, 0\n\t" \
  "s_waitcnt vmcnt(7)\n\t" \
  "s_barrier\n\t" \
  "ds_read_b128 v[72:75], v118 offset:5120\n\t" \
  "v_exp_f32 v41, v25\n\t" \
  "ds_read_b128 v[68:71], v116 offset:16672\n\t" \
  "v_exp_f32 v40, v24\n\t" \
  "v_exp_f32 v42, v26\n\t" \
  "v_exp_f32 v43, v27\n\t" \
  "ds_read_b128 v[64:67], v116 offset:16640\n\t" \
  "s_bitcmp1_b32 s41, s36\n\t" \
  "ds_read_b128 v[76:79], v118 offset:4096\n\t" \
  "s_cbranch_scc1 EV1_%=\n\t" \
  "R1_%=:\n\t" \
  "v_exp_f32 v44, v28\n\t" \
  "v_exp_f32 v45, v29\n\t" \
  "v_exp_f32 v46, v30\n\t" \
  "v_exp_f32 v47, v31\n\t" \
  "v_exp_f32 v34, v18\n\t" \
  "ds_read_b128 v[240:243], v112\n\t" \
  "ds_read_b128 v[244:247], v112 offset:16384\n\t" \
  "v_xor_b32 v117, 32, v112\n\t" \
  "ds_read_b128 v[248:251], v117\n\t" \
  "ds_read_b128 v[252:255], v117 offset:16384\n\t" \
  "v_pk_add_f32 v[46:47], v[46:47], 1.0 op_sel_hi:[1,0]\n\t" \
  "v_exp_f32 v32, v16\n\t" \
  "s_sub_u32 s48, 33280, s48\n\t" \
  "v_add_u32 v116, s48, v110\n\t" \
  "s_add_u32 s64, s58, 7168\n\t" \
  "s_addc_u32 s65, s59, 0\n\t" \
  "global_load_dwordx4 v[172:175], v111, s[64:65] sc1\n\t" \
  "v_pk_add_f32 v[44:45], v[44:45], 1.0 op_sel_hi:[1,0]\n\t" \
  "s_waitcnt vmcnt(7)\n\t" \
  "s_waitcnt lgkmcnt(3)\n\t" \
  "v_mfma_f32_32x32x16_bf16 v[208:223], v[144:147], v[240:243], 0\n\t" \
  "v_exp_f32 v36, v20\n\t" \
  "s_add_u32 s44, s44, 8192\n\t" \
  "v_pk_add_f32 v[40:41], v[40:41], 1.0 op_sel_hi:[1,0]\n\t" \
  "v_exp_f32 v38, v22\n\t" \
  "s_waitcnt lgkmcnt(2)\n\t" \
  "v_mfma_f32_32x32x16_bf16 v[224:239], v[244:247], v[144:147], 0\n\t" \
  "v_exp_f32 v37, v21\n\t" \
  "v_exp_f32 v35, v19\n\t" \
  "v_exp_f32 v33, v17\n\t" \
  "s_add_u32 s64, s58, 8192\n\t" \
  "s_addc_u32 s65, s59, 0\n\t" \
  "global_load_dwordx4 v[176:179], v111, s[64:65] sc1\n\t" \
  "v_exp_f32 v39, v23\n\t" \
  "s_waitcnt vmcnt(7)\n\t" \
  "s_waitcnt lgkmcnt(1)\n\t" \
  "v_mfma_f32_32x32x16_bf16 v[208:223], v[148:151], v[248:251], v[208:223]\n\t" \
  "v_pk_add_f32 v[32:33], v[32:33], 1.0 op_sel_hi:[1,0]\n\t" \
  "v_rcp_f32 v45, v45\n\t" \
  "v_pk_add_f32 v[42:43], v[42:43], 1.0 op_sel_hi:[1,0]\n\t" \
  "v_pk_add_f32 v[38:39], v[38:39], 1.0 op_sel_hi:[1,0]\n\t" \
  "v_pk_add_f32 v[34:35], v[34:35], 1.0 op_sel_hi:[1,0]\n\t" \
  "v_pk_add_f32 v[36:37], v[36:37], 1.0 op_sel_hi:[1,0]\n\t" \
  "s_add_u32 s64, s58, 9216\n\t" \
  "s_addc_u32 s65, s59, 0\n\t" \
  "global_load_dwordx4 v[180:183], v111, s[64:65] sc1\n\t" \
  "s_waitcnt lgkmcnt(4)\n\t" \
  "v_mfma_f32_32x32x16_bf16 v[0:15], v[64:67], v[80:83], 0\n\t" \
  "v_rcp_f32 v43, v43\n\t" \
  "v_rcp_f32 v33, v33\n\t" \
  "v_rcp_f32 v37, v37\n\t" \
  "v_mfma_f32_32x32x16_bf16 v[0:15], v[68:71], v[84:87], v[0:15]\n\t" \
  "ds_read_b128 v[64:67], v118 offset:6144\n\t" \
  "ds_read_b128 v[68:71], v118 offset:7168\n\t" \
  "v_rcp_f32 v35, v35\n\t" \
  "s_add_u32 s64, s58, 10240\n\t" \
  "s_addc_u32 s65, s59, 0\n\t" \
  "global_load_dwordx4 v[184:187], v111, s[64:65] sc1\n\t" \
  "v_rcp_f32 v38, v38\n\t" \
  "v_mfma_f32_32x32x16_bf16 v[0:15], v[72:75], v[88:91], v[0:15]\n\t" \
  "s_and_b32 s44, s44, 0x7fff\n\t" \
  "v_rcp_f32 v39, v39\n\t" \
  "v_rcp_f32 v36, v36\n\t" \
  "v_rcp_f32 v41, v41\n\t" \
  "v_rcp_f32 v34, v34\n\t" \
  "v_mfma_f32_32x32x16_bf16 v[0:15], v[76:79], v[96:99], v[0:15]\n\t" \
  "s_add_u32 s64, s58, 11264\n\t" \
  "s_addc_u32 s65, s59, 0\n\t" \
  "global_load_dwordx4 v[188:191], v111, s[64:65] sc1\n\t" \
  "v_rcp_f32 v32, v32\n\t" \
  "v_rcp_f32 v40, v40\n\t" \
  "v_rcp_f32 v47, v47\n\t" \
  "v_rcp_f32 v46, v46\n\t" \
  "v_rcp_f32 v42, v42\n\t" \
  "v_pk_mul_f32 v[32:33], v[32:33], v[16:17]\n\t" \
  "v_pk_mul_f32 v[34:35], v[34:35], v[18:19]\n\t" \
  "s_add_u32 s64, s58, 12288\n\t" \
  "s_addc_u32 s65, s59, 0\n\t" \
  "global_load_dwordx4 v[192:195], v111, s[64:65] sc1\n\t" \
  "v_pk_mul_f32 v[40:41], v[40:41], v[24:25]\n\t" \
  "v_pk_mul_f32 v[42:43], v[42:43], v[26:27]\n\t" \
  "v_pk_mul_f32 v[38:39], v[38:39], v[22:23]\n\t" \
  "s_waitcnt lgkmcnt(2)\n\t" \
  "v_mfma_f32_32x32x16_bf16 v[224:239], v[252:255], v[148:151], v[224:239]\n\t" \
  "v_cvt_pk_bf16_f32 v32, v32, v33\n\t" \
  "v_rcp_f32 v44, v44\n\t" \
  "v_pk_mul_f32 v[36:37], v[36:37], v[20:21]\n\t" \
  "v_add_u32 v118, s44, v125\n\t" \
  "v_cvt_pk_bf16_f32 v33, v34, v35\n\t" \
  "s_add_u32 s64, s58, 13312\n\t" \
  "s_addc_u32 s65, s59, 0\n\t" \
  "global_load_dwordx4 v[196:199], v111, s[64:65] sc1\n\t" \
  "v_cvt_pk_bf16_f32 v34, v36, v37\n\t" \
  "v_cvt_pk_bf16_f32 v35, v38, v39\n\t" \
  "v_pk_mul_f32 v[46:47], v[46:47], v[30:31]\n\t" \
  "v_pk_mul_f32 v[44:45], v[44:45], v[28:29]\n\t" \
  "s_waitcnt lgkmcnt(1)\n\t" \
  "v_mfma_f32_32x32x16_bf16 v[48:63], v[32:35], v[64:67], v[48:63]\n\t" \
  "v_cvt_pk_bf16_f32 v38, v44, v45\n\t" \
  "v_cvt_pk_bf16_f32 v36, v40, v41\n\t" \
  "v_cvt_pk_bf16_f32 v37, v42, v43\n\t" \
  "v_cvt_pk_bf16_f32 v39, v46, v47\n\t" \
  "s_add_u32 s36, s36, 1\n\t" \
  "s_waitcnt lgkmcnt(0)\n\t" \
  "v_mfma_f32_32x32x16_bf16 v[48:63], v[36:39], v[68:71], v[48:63]\n\t" \
  "s_getpc_b64 s[68:69]\n\t" \
  "PCe_%=:\n\t" \
  "s_add_u32 s68, s68, R2_%=-PCe_%=\n\t" \
  "s_addc_u32 s69, s69, 0\n\t" \
  "s_nop 0\n\t" \
  "s_barrier\n\t" \
  "ds_read_b128 v[72:75], v118 offset:1024\n\t" \
  "v_exp_f32 v41, v9\n\t" \
  "ds_read_b128 v[68:71], v116 offset:32\n\t" \
  "v_exp_f32 v40, v8\n\t" \
  "v_exp_f32 v42, v10\n\t" \
  "v_exp_f32 v43, v11\n\t" \
  "ds_read_b128 v[64:67], v116 offset:0\n\t" \
  "s_bitcmp1_b32 s41, s36\n\t" \
  "ds_read_b128 v[76:79], v118 offset:0\n\t" \
  "s_cbranch_scc1 EV0_%=\n\t" \
  "R2_%=:\n\t" \
  "v_exp_f32 v44, v12\n\t" \
  "v_exp_f32 v45, v13\n\t" \
  "v_exp_f32 v46, v14\n\t" \
  "v_exp_f32 v47, v15\n\t" \
  "v_exp_f32 v34, v2\n\t" \
  "v_xor_b32 v126, 64, v112\n\t" \
  "ds_read_b128 v[240:243], v126\n\t" \
  "ds_read_b128 v[244:247], v126 offset:16384\n\t" \
  "v_xor_b32 v117, 96, v112\n\t" \
  "ds_read_b128 v[248:251], v117\n\t" \
  "ds_read_b128 v[252:255], v117 offset:16384\n\t" \
  "v_pk_add_f32 v[46:47], v[46:47], 1.0 op_sel_hi:[1,0]\n\t" \
  "v_exp_f32 v32, v0\n\t" \
  "v_pk_add_f32 v[44:45], v[44:45], 1.0 op_sel_hi:[1,0]\n\t" \
  "v_exp_f32 v36, v4\n\t" \
  "v_pk_add_f32 v[40:41], v[40:41], 1.0 op_sel_hi:[1,0]\n\t" \
  "s_waitcnt vmcnt(11)\n\t" \
  "s_waitcnt lgkmcnt(3)\n\t" \
  "v_mfma_f32_32x32x16_bf16 v[208:223], v[152:155], v[240:243], v[208:223]\n\t" \
  "v_exp_f32 v38, v6\n\t" \
  "v_exp_f32 v37, v5\n\t" \
  "s_add_u32 s64, s58, 14336\n\t" \
  "s_addc_u32 s65, s59, 0\n\t" \
  "global_load_dwordx4 v[200:203], v111, s[64:65] sc1\n\t" \
  "v_exp_f32 v35, v3\n\t" \
  "v_exp_f32 v33, v1\n\t" \
  "s_waitcnt lgkmcnt(2)\n\t" \
  "v_mfma_f32_32x32x16_bf16 v[224:239], v[244:247], v[152:155], v[224:239]\n\t" \
  "v_exp_f32 v39, v7\n\t" \
  "v_pk_add_f32 v[32:33], v[32:33], 1.0 op_sel_hi:[1,0]\n\t" \
  "v_rcp_f32 v45, v45\n\t" \
  "v_pk_add_f32 v[42:43], v[42:43], 1.0 op_sel_hi:[1,0]\n\t" \
  "s_waitcnt vmcnt(11)\n\t" \
  "s_waitcnt lgkmcnt(1)\n\t" \
  "v_mfma_f32_32x32x16_bf16 v[208:223], v[156:159], v[248:251], v[208:223]\n\t" \
  "v_pk_add_f32 v[38:39], v[38:39], 1.0 op_sel_hi:[1,0]\n\t" \
  "v_pk_add_f32 v[34:35], v[34:35], 1.0 op_sel_hi:[1,0]\n\t" \
  "v_pk_add_f32 v[36:37], v[36:37], 1.0 op_sel_hi:[1,0]\n\t" \
  "s_waitcnt lgkmcnt(4)\n\t" \
  "v_mfma_f32_32x32x16_bf16 v[16:31], v[64:67], v[80:83], 0\n\t" \
  "v_rcp_f32 v43, v43\n\t" \
  "v_rcp_f32 v33, v33\n\t" \
  "s_add_u32 s64, s58, 15360\n\t" \
  "s_addc_u32 s65, s59, 0\n\t" \
  "global_load_dwordx4 v[204:207], v111, s[64:65] sc1\n\t" \
  "v_rcp_f32 v37, v37\n\t" \
  "v_mfma_f32_32x32x16_bf16 v[16:31], v[68:71], v[84:87], v[16:31]\n\t" \
  "ds_read_b128 v[64:67], v118 offset:2048\n\t" \
  "ds_read_b128 v[68:71], v118 offset:3072\n\t" \
  "v_rcp_f32 v35, v35\n\t" \
  "v_rcp_f32 v38, v38\n\t" \
  "v_mfma_f32_32x32x16_bf16 v[16:31], v[72:75], v[88:91], v[16:31]\n\t" \
  "v_rcp_f32 v39, v39\n\t" \
  "v_rcp_f32 v36, v36\n\t" \
  "v_rcp_f32 v41, v41\n\t" \
  "v_rcp_f32 v34, v34\n\t" \
  "v_mfma_f32_32x32x16_bf16 v[16:31], v[76:79], v[96:99], v[16:31]\n\t" \
  "s_add_u32 s64, s58, 16384\n\t" \
  "s_addc_u32 s65, s59, 0\n\t" \
  "global_load_dwordx4 v[144:147], v111, s[64:65] sc1\n\t" \
  "v_rcp_f32 v32, v32\n\t" \
  "v_rcp_f32 v40, v40\n\t" \
  "v_rcp_f32 v47, v47\n\t" \
  "v_rcp_f32 v46, v46\n\t" \
  "v_rcp_f32 v42, v42\n\t" \
  "v_pk_mul_f32 v[32:33], v[32:33], v[0:1]\n\t" \
  "v_pk_mul_f32 v[34:35], v[34:35], v[2:3]\n\t" \
  "v_pk_mul_f32 v[40:41], v[40:41], v[8:9]\n\t" \
  "v_pk_mul_f32 v[42:43], v[42:43], v[10:11]\n\t" \
  "s_waitcnt lgkmcnt(2)\n\t" \
  "v_mfma_f32_32x32x16_bf16 v[224:239], v[252:255], v[156:159], v[224:239]\n\t" \
  "v_pk_mul_f32 v[38:39], v[38:39], v[6:7]\n\t" \
  "v_cvt_pk_bf16_f32 v32, v32, v33\n\t" \
  "v_rcp_f32 v44, v44\n\t" \
  "s_add_u32 s64, s58, 17408\n\t" \
  "s_addc_u32 s65, s59, 0\n\t" \
  "global_load_dwordx4 v[148:151], v111, s[64:65] sc1\n\t" \
  "v_pk_mul_f32 v[36:37], v[36:37], v[4:5]\n\t" \
  "v_cvt_pk_bf16_f32 v33, v34, v35\n\t" \
  "v_cvt_pk_bf16_f32 v34, v36, v37\n\t" \
  "v_cvt_pk_bf16_f32 v35, v38, v39\n\t" \
  "v_pk_mul_f32 v[46:47], v[46:47], v[14:15]\n\t" \
  "v_pk_mul_f32 v[44:45], v[44:45], v[12:13]\n\t" \
  "s_waitcnt lgkmcnt(1)\n\t" \
  "v_mfma_f32_32x32x16_bf16 v[48:63], v[32:35], v[64:67], v[48:63]\n\t" \
  "v_cvt_pk_bf16_f32 v38, v44, v45\n\t" \
  "v_cvt_pk_bf16_f32 v36, v40, v41\n\t" \
  "v_cvt_pk_bf16_f32 v37, v42, v43\n\t" \
  "v_cvt_pk_bf16_f32 v39, v46, v47\n\t" \
  "s_add_u32 s36, s36, 1\n\t" \
  "s_waitcnt lgkmcnt(0)\n\t" \
  "v_mfma_f32_32x32x16_bf16 v[48:63], v[36:39], v[68:71], v[48:63]\n\t" \
  "s_getpc_b64 s[70:71]\n\t" \
  "PCf_%=:\n\t" \
  "s_add_u32 s70, s70, R3_%=-PCf_%=\n\t" \
  "s_addc_u32 s71, s71, 0\n\t" \
  "s_nop 0\n\t" \
  "s_barrier\n\t" \
  "v_exp_f32 v41, v25\n\t" \
  "v_exp_f32 v40, v24\n\t" \
  "v_exp_f32 v42, v26\n\t" \
  "v_exp_f32 v43, v27\n\t" \
  "s_bitcmp1_b32 s41, s36\n\t" \
  "s_cbranch_scc1 EV1_%=\n\t" \
  "R3_%=:\n\t" \
  "v_exp_f32 v44, v28\n\t" \
  "v_exp_f32 v45, v29\n\t" \
  "v_exp_f32 v46, v30\n\t" \
  "v_exp_f32 v47, v31\n\t" \
  "v_exp_f32 v34, v18\n\t" \
  "v_xor_b32 v126, 128, v112\n\t" \
  "ds_read_b128 v[240:243], v126\n\t" \
  "ds_read_b128 v[244:247], v126 offset:16384\n\t" \
  "v_xor_b32 v117, 160, v112\n\t" \
  "ds_read_b128 v[248:251], v117\n\t" \
  "ds_read_b128 v[252:255], v117 offset:16384\n\t" \
  "v_pk_add_f32 v[46:47], v[46:47], 1.0 op_sel_hi:[1,0]\n\t" \
  "v_exp_f32 v32, v16\n\t" \
  "v_pk_add_f32 v[44:45], v[44:45], 1.0 op_sel_hi:[1,0]\n\t" \
  "s_add_u32 s64, s58, 18432\n\t" \
  "s_addc_u32 s65, s59, 0\n\t" \
  "global_load_dwordx4 v[152:155], v111, s[64:65] sc1\n\t" \
  "v_exp_f32 v36, v20\n\t" \
  "v_pk_add_f32 v[40:41], v[40:41], 1.0 op_sel_hi:[1,0]\n\t" \
  "s_waitcnt vmcnt(14)\n\t" \
  "s_waitcnt lgkmcnt(3)\n\t" \
  "v_mfma_f32_32x32x16_bf16 v[208:223], v[160:163], v[240:243], v[208:223]\n\t" \
  "v_exp_f32 v38, v22\n\t" \
  "v_exp_f32 v37, v21\n\t" \
  "s_add_u32 s64, s58, 19456\n\t" \
  "s_addc_u32 s65, s59, 0\n\t" \
  "global_load_dwordx4 v[156:159], v111, s[64:65] sc1\n\t" \
  "v_exp_f32 v35, v19\n\t" \
  "s_waitcnt lgkmcnt(2)\n\t" \
  "v_mfma_f32_32x32x16_bf16 v[224:239], v[244:247], v[160:163], v[224:239]\n\t" \
  "v_xor_b32 v126, 192, v112\n\t" \
  "ds_read_b128 v[240:243], v126\n\t" \
  "ds_read_b128 v[244:247], v126 offset:16384\n\t" \
  "v_exp_f32 v33, v17\n\t" \
  "v_exp_f32 v39, v23\n\t" \
  "v_pk_add_f32 v[32:33], v[32:33], 1.0 op_sel_hi:[1,0]\n\t" \
  "s_waitcnt vmcnt(14)\n\t" \
  "s_waitcnt lgkmcnt(3)\n\t" \
  "v_mfma_f32_32x32x16_bf16 v[208:223], v[164:167], v[248:251], v[208:223]\n\t" \
  "v_rcp_f32 v45, v45\n\t" \
  "s_add_u32 s64, s58, 20480\n\t" \
  "s_addc_u32 s65, s59, 0\n\t" \
  "global_load_dwordx4 v[80:83], v111, s[64:65] sc1\n\t" \
  "v_pk_add_f32 v[42:43], v[42:43], 1.0 op_sel_hi:[1,0]\n\t" \
  "v_pk_add_f32 v[38:39], v[38:39], 1.0 op_sel_hi:[1,0]\n\t" \
  "s_waitcnt lgkmcnt(2)\n\t" \
  "v_mfma_f32_32x32x16_bf16 v[224:239], v[252:255], v[164:167], v[224:239]\n\t" \
  "v_xor_b32 v117, 224, v112\n\t" \
  "ds_read_b128 v[248:251], v117\n\t" \
  "ds_read_b128 v[252:255], v117 offset:16384\n\t" \
  "v_pk_add_f32 v[34:35], v[34:35], 1.0 op_sel_hi:[1,0]\n\t" \
  "v_pk_add_f32 v[36:37], v[36:37], 1.0 op_sel_hi:[1,0]\n\t" \
  "s_add_u32 s64, s58, 21504\n\t" \
  "s_addc_u32 s65, s59, 0\n\t" \
  "global_load_dwordx4 v[84:87], v111, s[64:65] sc1\n\t" \
  "v_rcp_f32 v43, v43\n\t" \
  "s_waitcnt vmcnt(15)\n\t" \
  "s_waitcnt lgkmcnt(3)\n\t" \
  "v_mfma_f32_32x32x16_bf16 v[208:223], v[168:171], v[240:243], v[208:223]\n\t" \
  "v_rcp_f32 v33, v33\n\t" \
  "v_rcp_f32 v37, v37\n\t" \
  "ds_read_b128 v[64:67], v118 offset:6144\n\t" \
  "s_waitcnt lgkmcnt(3)\n\t" \
  "v_mfma_f32_32x32x16_bf16 v[224:239], v[244:247], v[168:171], v[224:239]\n\t" \
  "v_xor_b32 v126, 256, v112\n\t" \
  "ds_read_b128 v[240:243], v126\n\t" \
  "ds_read_b128 v[244:247], v126 offset:16384\n\t" \
  "s_add_u32 s64, s58, 22528\n\t" \
  "s_addc_u32 s65, s59, 0\n\t" \
  "global_load_dwordx4 v[88:91], v111, s[64:65] sc1\n\t" \
  "ds_read_b128 v[68:71], v118 offset:7168\n\t" \
  "v_rcp_f32 v35, v35\n\t" \
  "v_rcp_f32 v38, v38\n\t" \
  "v_rcp_f32 v39, v39\n\t" \
  "s_waitcnt vmcnt(15)\n\t" \
  "s_waitcnt lgkmcnt(5)\n\t" \
  "v_mfma_f32_32x32x16_bf16 v[208:223], v[172:175], v[248:251], v[208:223]\n\t" \
  "v_rcp_f32 v36, v36\n\t" \
  "s_add_u32 s64, s58, 23552\n\t" \
  "s_addc_u32 s65, s59, 0\n\t" \
  "global_load_dwordx4 v[92:95], v111, s[64:65] sc1\n\t" \
  "v_rcp_f32 v41, v41\n\t" \
  "v_rcp_f32 v34, v34\n\t" \
  "s_waitcnt lgkmcnt(4)\n\t" \
  "v_mfma_f32_32x32x16_bf16 v[224:239], v[252:255], v[172:175], v[224:239]\n\t" \
  "v_xor_b32 v117, 288, v112\n\t" \
  "ds_read_b128 v[248:251], v117\n\t" \
  "ds_read_b128 v[252:255], v117 offset:16384\n\t" \
  "v_rcp_f32 v32, v32\n\t" \
  "v_rcp_f32 v40, v40\n\t" \
  "s_add_u32 s64, s58, 24576\n\t" \
  "s_addc_u32 s65, s59, 0\n\t" \
  "global_load_dwordx4 v[0:3], v111, s[64:65] sc1\n\t" \
  "v_rcp_f32 v47, v47\n\t" \
  "s_waitcnt vmcnt(16)\n\t" \
  "s_waitcnt lgkmcnt(4)\n\t" \
  "v_mfma_f32_32x32x16_bf16 v[208:223], v[176:179], v[240:243], v[208:223]\n\t" \
  "v_rcp_f32 v46, v46\n\t" \
  "v_rcp_f32 v42, v42\n\t" \
  "v_pk_mul_f32 v[32:33], v[32:33], v[16:17]\n\t" \
  "s_waitcnt lgkmcnt(3)\n\t" \
  "v_mfma_f32_32x32x16_bf16 v[224:239], v[244:247], v[176:179], v[224:239]\n\t" \
  "v_pk_mul_f32 v[34:35], v[34:35], v[18:19]\n\t" \
  "s_add_u32 s64, s58, 25600\n\t" \
  "s_addc_u32 s65, s59, 0\n\t" \
  "global_load_dwordx4 v[4:7], v111, s[64:65] sc1\n\t" \
  "v_pk_mul_f32 v[40:41], v[40:41], v[24:25]\n\t" \
  "v_pk_mul_f32 v[42:43], v[42:43], v[26:27]\n\t" \
  "s_waitcnt vmcnt(16)\n\t" \
  "s_waitcnt lgkmcnt(1)\n\t" \
  "v_mfma_f32_32x32x16_bf16 v[208:223], v[180:183], v[248:251], v[208:223]\n\t" \
  "v_pk_mul_f32 v[38:39], v[38:39], v[22:23]\n\t" \
  "v_cvt_pk_bf16_f32 v32, v32, v33\n\t" \
  "s_add_u32 s64, s58, 26624\n\t" \
  "s_addc_u32 s65, s59, 0\n\t" \
  "global_load_dwordx4 v[8:11], v111, s[64:65] sc1\n\t" \
  "v_rcp_f32 v44, v44\n\t" \
  "s_waitcnt lgkmcnt(0)\n\t" \
  "v_mfma_f32_32x32x16_bf16 v[224:239], v[252:255], v[180:183], v[224:239]\n\t" \
  "v_pk_mul_f32 v[36:37], v[36:37], v[20:21]\n\t" \
  "v_cvt_pk_bf16_f32 v33, v34, v35\n\t" \
  "v_cvt_pk_bf16_f32 v34, v36, v37\n\t" \
  "s_add_u32 s64, s58, 27648\n\t" \
  "s_addc_u32 s65, s59, 0\n\t" \
  "global_load_dwordx4 v[12:15], v111, s[64:65] sc1\n\t" \
  "v_cvt_pk_bf16_f32 v35, v38, v39\n\t" \
  "v_pk_mul_f32 v[46:47], v[46:47], v[30:31]\n\t" \
  "v_pk_mul_f32 v[44:45], v[44:45], v[28:29]\n\t" \
  "s_waitcnt lgkmcnt(5)\n\t" \
  "v_mfma_f32_32x32x16_bf16 v[48:63], v[32:35], v[64:67], v[48:63]\n\t" \
  "v_cvt_pk_bf16_f32 v38, v44, v45\n\t" \
  "v_cvt_pk_bf16_f32 v36, v40, v41\n\t" \
  "v_cvt_pk_bf16_f32 v37, v42, v43\n\t" \
  "v_cvt_pk_bf16_f32 v39, v46, v47\n\t" \
  "s_add_u32 s36, s36, 1\n\t" \
  "s_waitcnt lgkmcnt(2)\n\t" \
  "v_mfma_f32_32x32x16_bf16 v[48:63], v[36:39], v[68:71], v[48:63]\n\t" \
  "s_add_u32 s64, s58, 28672\n\t" \
  "s_addc_u32 s65, s59, 0\n\t" \
  "global_load_dwordx4 v[160:163], v111, s[64:65] sc1\n\t" \
  "s_add_u32 s64, s58, 29696\n\t" \
  "s_addc_u32 s65, s59, 0\n\t" \
  "global_load_dwordx4 v[164:167], v111, s[64:65] sc1\n\t" \
  "s_add_u32 s64, s58, 30720\n\t" \
  "s_addc_u32 s65, s59, 0\n\t" \
  "global_load_dwordx4 v[168:171], v111, s[64:65] sc1\n\t" \
  "s_add_u32 s64, s58, 31744\n\t" \
  "s_addc_u32 s65, s59, 0\n\t" \
  "global_load_dwordx4 v[172:175], v111, s[64:65] sc1\n\t" \
  "s_bitcmp1_b32 s42, s36\n\t" \
  "s_cbranch_scc0 R4_%=\n\t" \
  "ds_read_b128 v[24:27], v122\n\t" \
  "ds_read_b128 v[28:31], v122 offset:1024\n\t" \
  "v_cvt_pk_bf16_f32 v16, v48, v49\n\t" \
  "v_cvt_pk_bf16_f32 v17, v50, v51\n\t" \
  "v_cvt_pk_bf16_f32 v18, v52, v53\n\t" \
  "v_cvt_pk_bf16_f32 v19, v54, v55\n\t" \
  "v_cvt_pk_bf16_f32 v20, v56, v57\n\t" \
  "v_cvt_pk_bf16_f32 v21, v58, v59\n\t" \
  "v_cvt_pk_bf16_f32 v22, v60, v61\n\t" \
  "v_cvt_pk_bf16_f32 v23, v62, v63\n\t" \
  "ds_write2_b64 v120, v[16:17], v[18:19] offset0:0 offset1:2\n\t" \
  "ds_write2_b64 v120, v[20:21], v[22:23] offset0:4 offset1:6\n\t" \
  "v_pk_mul_f32 v[48:49], v[48:49], v[128:129]\n\t" \
  "v_pk_mul_f32 v[50:51], v[50:51], v[130:131]\n\t" \
  "v_pk_mul_f32 v[52:53], v[52:53], v[132:133]\n\t" \
  "v_pk_mul_f32 v[54:55], v[54:55], v[134:135]\n\t" \
  "v_add_u32 v120, 3696, v120\n\t" \
  "v_add_u32 v122, 2048, v122\n\t" \
  "v_pk_fma_f32 v[16:17], v[56:57], v[136:137], v[48:49]\n\t" \
  "v_pk_fma_f32 v[18:19], v[58:59], v[138:139], v[50:51]\n\t" \
  "v_pk_fma_f32 v[20:21], v[60:61], v[140:141], v[52:53]\n\t" \
  "v_pk_fma_f32 v[22:23], v[62:63], v[142:143], v[54:55]\n\t" \
  "s_waitcnt lgkmcnt(2)\n\t" \
  "v_mfma_f32_32x32x16_bf16 v[48:63], v[32:35], v[24:27], 0\n\t" \
  "v_mov_b64 v[96:97], v[98:99]\n\t" \
  "v_mov_b64 v[98:99], v[100:101]\n\t" \
  "v_pk_add_f32 v[16:17], v[16:17], v[18:19]\n\t" \
  "v_pk_add_f32 v[20:21], v[20:21], v[22:23]\n\t" \
  "v_mov_b64 v[100:101], v[102:103]\n\t" \
  "v_mov_b64 v[102:103], v[104:105]\n\t" \
  "v_pk_add_f32 v[16:17], v[16:17], v[20:21]\n\t" \
  "v_mfma_f32_32x32x16_bf16 v[48:63], v[36:39], v[28:31], v[48:63]\n\t" \
  "v_mov_b64 v[104:105], v[106:107]\n\t" \
  "v_mov_b64 v[106:107], v[108:109]\n\t" \
  "v_add_f32 v16, v16, v17\n\t" \
  "ds_write_b32 v121, v16\n\t" \
  "v_add_u32 v121, 2048, v121\n\t" \
  "s_nop 7\n\t" \
  "s_nop 3\n\t" \
  "R4_%=:\n\t" \
  "v_xor_b32 v126, 320, v112\n\t" \
  "ds_read_b128 v[240:243], v126\n\t" \
  "ds_read_b128 v[244:247], v126 offset:16384\n\t" \
  "v_xor_b32 v117, 352, v112\n\t" \
  "ds_read_b128 v[248:251], v117\n\t" \
  "ds_read_b128 v[252:255], v117 offset:16384\n\t" \
  "s_waitcnt vmcnt(16)\n\t" \
  "v_cvt_pk_bf16_f32 v16, v48, v49\n\t" \
  "v_cvt_pk_bf16_f32 v17, v50, v51\n\t" \
  "v_cvt_pk_bf16_f32 v18, v52, v53\n\t" \
  "v_cvt_pk_bf16_f32 v19, v54, v55\n\t" \
  "s_waitcnt lgkmcnt(3)\n\t" \
  "v_mfma_f32_32x32x16_bf16 v[208:223], v[184:187], v[240:243], v[208:223]\n\t" \
  "v_cvt_pk_bf16_f32 v20, v56, v57\n\t" \
  "v_cvt_pk_bf16_f32 v21, v58, v59\n\t" \
  "s_waitcnt lgkmcnt(2)\n\t" \
  "v_mfma_f32_32x32x16_bf16 v[224:239], v[244:247], v[184:187], v[224:239]\n\t" \
  "v_xor_b32 v126, 384, v112\n\t" \
  "ds_read_b128 v[240:243], v126\n\t" \
  "ds_read_b128 v[244:247], v126 offset:16384\n\t" \
  "v_cvt_pk_bf16_f32 v22, v60, v61\n\t" \
  "v_cvt_pk_bf16_f32 v23, v62, v63\n\t" \
  "s_waitcnt lgkmcnt(3)\n\t" \
  "v_mfma_f32_32x32x16_bf16 v[208:223], v[188:191], v[248:251], v[208:223]\n\t" \
  "ds_write2_b64 v120, v[16:17], v[18:19] offset0:0 offset1:2\n\t" \
  "ds_write2_b64 v120, v[20:21], v[22:23] offset0:4 offset1:6\n\t" \
  "s_waitcnt lgkmcnt(4)\n\t" \
  "v_mfma_f32_32x32x16_bf16 v[224:239], v[252:255], v[188:191], v[224:239]\n\t" \
  "v_xor_b32 v117, 416, v112\n\t" \
  "ds_read_b128 v[248:251], v117\n\t" \
  "ds_read_b128 v[252:255], v117 offset:16384\n\t" \
  "v_pk_mul_f32 v[48:49], v[48:49], v[128:129]\n\t" \
  "v_pk_mul_f32 v[50:51], v[50:51], v[130:131]\n\t" \
  "s_waitcnt lgkmcnt(5)\n\t" \
  "v_mfma_f32_32x32x16_bf16 v[208:223], v[192:195], v[240:243], v[208:223]\n\t" \
  "v_pk_mul_f32 v[52:53], v[52:53], v[132:133]\n\t" \
  "v_pk_mul_f32 v[54:55], v[54:55], v[134:135]\n\t" \
  "s_waitcnt lgkmcnt(4)\n\t" \
  "v_mfma_f32_32x32x16_bf16 v[224:239], v[244:247], v[192:195], v[224:239]\n\t" \
  "v_xor_b32 v126, 448, v112\n\t" \
  "ds_read_b128 v[240:243], v126\n\t" \
  "ds_read_b128 v[244:247], v126 offset:16384\n\t" \
  "v_pk_fma_f32 v[16:17], v[56:57], v[136:137], v[48:49]\n\t" \
  "v_pk_fma_f32 v[18:19], v[58:59], v[138:139], v[50:51]\n\t" \
  "s_waitcnt lgkmcnt(3)\n\t" \
  "v_mfma_f32_32x32x16_bf16 v[208:223], v[196:199], v[248:251], v[208:223]\n\t" \
  "v_pk_fma_f32 v[20:21], v[60:61], v[140:141], v[52:53]\n\t" \
  "v_pk_fma_f32 v[22:23], v[62:63], v[142:143], v[54:55]\n\t" \
  "s_waitcnt lgkmcnt(2)\n\t" \
  "v_mfma_f32_32x32x16_bf16 v[224:239], v[252:255], v[196:199], v[224:239]\n\t" \
  "v_xor_b32 v117, 480, v112\n\t" \
  "ds_read_b128 v[248:251], v117\n\t" \
  "ds_read_b128 v[252:255], v117 offset:16384\n\t" \
  "v_pk_add_f32 v[16:17], v[16:17], v[18:19]\n\t" \
  "v_pk_add_f32 v[20:21], v[20:21], v[22:23]\n\t" \
  "s_waitcnt lgkmcnt(3)\n\t" \
  "v_mfma_f32_32x32x16_bf16 v[208:223], v[200:203], v[240:243], v[208:223]\n\t" \
  "v_pk_add_f32 v[16:17], v[16:17], v[20:21]\n\t" \
  "v_add_f32 v16, v16, v17\n\t" \
  "s_waitcnt lgkmcnt(2)\n\t" \
  "v_mfma_f32_32x32x16_bf16 v[224:239], v[244:247], v[200:203], v[224:239]\n\t" \
  "ds_write_b32 v121, v16\n\t" \
  "s_waitcnt lgkmcnt(2)\n\t" \
  "v_mfma_f32_32x32x16_bf16 v[208:223], v[204:207], v[248:251], v[208:223]\n\t" \
  "s_waitcnt lgkmcnt(1)\n\t" \
  "v_mfma_f32_32x32x16_bf16 v[224:239], v[252:255], v[204:207], v[224:239]\n\t" \
  "s_waitcnt lgkmcnt(0)\n\t" \
  "s_barrier\n\t" \
  "ds_read_b128 v[16:19], v114 offset:0\n\t" \
  "ds_read_b128 v[20:23], v114 offset:32\n\t" \
  "ds_read_b128 v[24:27], v114 offset:64\n\t" \
  "ds_read_b128 v[28:31], v114 offset:96\n\t" \
  "s_mov_b32 s78, 0xbfb8aa3b\n\t" \
  "s_mov_b32 s79, 0xbfb8aa3b\n\t" \
  "s_lshl_b32 s66, s61, 6\n\t" \
  "v_xor_b32 v117, s66, v112\n\t" \
  "v_xor_b32 v126, 32, v117\n\t" \
  "v_pk_mul_f32 v[208:209], v[208:209], s[78:79]\n\t" \
  "v_pk_mul_f32 v[210:211], v[210:211], s[78:79]\n\t" \
  "v_pk_mul_f32 v[212:213], v[212:213], s[78:79]\n\t" \
  "v_pk_mul_f32 v[214:215], v[214:215], s[78:79]\n\t" \
  "v_pk_mul_f32 v[216:217], v[216:217], s[78:79]\n\t" \
  "v_pk_mul_f32 v[218:219], v[218:219], s[78:79]\n\t" \
  "v_pk_mul_f32 v[220:221], v[220:221], s[78:79]\n\t" \
  "v_pk_mul_f32 v[222:223], v[222:223], s[78:79]\n\t" \
  "v_pk_mul_f32 v[224:225], v[224:225], s[78:79]\n\t" \
  "v_pk_mul_f32 v[226:227], v[226:227], s[78:79]\n\t" \
  "s_waitcnt vmcnt(4)\n\t" \
  "s_waitcnt lgkmcnt(3)\n\t" \
  "v_mfma_f32_32x32x16_bf16 v[208:223], v[144:147], v[16:19], v[208:223]\n\t" \
  "ds_read_b128 v[32:35], v114 offset:128\n\t" \
  "s_waitcnt lgkmcnt(3)\n\t" \
  "v_mfma_f32_32x32x16_bf16 v[208:223], v[148:151], v[20:23], v[208:223]\n\t" \
  "ds_read_b128 v[36:39], v114 offset:160\n\t" \
  "ds_read_b128 v[96:99], v117\n\t" \
  "s_waitcnt lgkmcnt(4)\n\t" \
  "v_mfma_f32_32x32x16_bf16 v[208:223], v[152:155], v[24:27], v[208:223]\n\t" \
  "ds_read_b128 v[40:43], v114 offset:192\n\t" \
  "ds_read_b128 v[100:103], v126\n\t" \
  "s_waitcnt lgkmcnt(5)\n\t" \
  "v_mfma_f32_32x32x16_bf16 v[208:223], v[156:159], v[28:31], v[208:223]\n\t" \
  "ds_read_b128 v[44:47], v114 offset:224\n\t" \
  "v_bfe_u32 v108, v111, 4, 5\n\t" \
  "v_and_b32 v109, 0x13, v108\n\t" \
  "s_waitcnt lgkmcnt(5)\n\t" \
  "v_mfma_f32_32x32x16_bf16 v[208:223], v[80:83], v[32:35], v[208:223]\n\t" \
  "ds_read_b128 v[16:19], v114 offset:256\n\t" \
  "v_bfe_u32 v127, v108, 2, 1\n\t" \
  "v_lshl_or_b32 v109, v127, 3, v109\n\t" \
  "s_waitcnt lgkmcnt(5)\n\t" \
  "v_mfma_f32_32x32x16_bf16 v[208:223], v[84:87], v[36:39], v[208:223]\n\t" \
  "ds_read_b128 v[20:23], v114 offset:288\n\t" \
  "v_bfe_u32 v127, v108, 3, 1\n\t" \
  "v_lshl_or_b32 v109, v127, 2, v109\n\t" \
  "s_waitcnt lgkmcnt(4)\n\t" \
  "v_mfma_f32_32x32x16_bf16 v[208:223], v[88:91], v[40:43], v[208:223]\n\t" \
  "ds_read_b128 v[24:27], v114 offset:320\n\t" \
  "v_lshlrev_b32 v109, 1, v109\n\t" \
  "v_lshrrev_b32 v108, 9, v111\n\t" \
  "s_waitcnt lgkmcnt(3)\n\t" \
  "v_mfma_f32_32x32x16_bf16 v[208:223], v[92:95], v[44:47], v[208:223]\n\t" \
  "ds_read_b128 v[28:31], v114 offset:352\n\t" \
  "v_lshl_add_u32 v116, v108, 19, v109\n\t" \
  "v_mul_u32_u24 v127, 2112, v108\n\t" \
  "s_waitcnt lgkmcnt(3)\n\t" \
  "v_mfma_f32_32x32x16_bf16 v[208:223], v[0:3], v[16:19], v[208:223]\n\t" \
  "ds_read_b128 v[32:35], v114 offset:384\n\t" \
  "v_add_u32 v109, s66, v109\n\t" \
  "v_xor_b32 v127, v127, v109\n\t" \
  "s_waitcnt lgkmcnt(3)\n\t" \
  "v_mfma_f32_32x32x16_bf16 v[208:223], v[4:7], v[20:23], v[208:223]\n\t" \
  "ds_read_b128 v[36:39], v114 offset:416\n\t" \
  "v_add_u32 v127, 150528, v127\n\t" \
  "ds_read_u16_d16_hi v104, v127\n\t" \
  "s_waitcnt lgkmcnt(4)\n\t" \
  "v_mfma_f32_32x32x16_bf16 v[208:223], v[8:11], v[24:27], v[208:223]\n\t" \
  "ds_read_b128 v[40:43], v114 offset:448\n\t" \
  "v_xor_b32 v108, 16, v127\n\t" \
  "ds_read_u16_d16_hi v105, v108 offset:512\n\t" \
  "s_waitcnt lgkmcnt(5)\n\t" \
  "v_mfma_f32_32x32x16_bf16 v[208:223], v[12:15], v[28:31], v[208:223]\n\t" \
  "ds_read_b128 v[44:47], v114 offset:480\n\t" \
  "v_xor_b32 v108, 32, v127\n\t" \
  "ds_read_u16_d16_hi v106, v108 offset:1024\n\t" \
  "s_waitcnt vmcnt(0)\n\t" \
  "s_waitcnt lgkmcnt(6)\n\t" \
  "v_mfma_f32_32x32x16_bf16 v[208:223], v[160:163], v[32:35], v[208:223]\n\t" \
  "ds_read_b128 v[48:51], v114 offset:16896\n\t" \
  "v_xor_b32 v108, 48, v127\n\t" \
  "ds_read_u16_d16_hi v107, v108 offset:1536\n\t" \
  "s_waitcnt lgkmcnt(7)\n\t" \
  "v_mfma_f32_32x32x16_bf16 v[208:223], v[164:167], v[36:39], v[208:223]\n\t" \
  "ds_read_b128 v[52:55], v114 offset:16928\n\t" \
  "s_waitcnt lgkmcnt(6)\n\t" \
  "v_mfma_f32_32x32x16_bf16 v[208:223], v[168:171], v[40:43], v[208:223]\n\t" \
  "ds_read_b128 v[56:59], v114 offset:16960\n\t" \
  "s_waitcnt lgkmcnt(5)\n\t" \
  "v_mfma_f32_32x32x16_bf16 v[208:223], v[172:175], v[44:47], v[208:223]\n\t" \
  "ds_read_b128 v[60:63], v114 offset:16992\n\t" \
  "s_waitcnt lgkmcnt(15)\n\t" \
  "s_waitcnt lgkmcnt(4)\n\t" \
  "v_mfma_f32_32x32x16_bf16 v[224:239], v[48:51], v[144:147], v[224:239]\n\t" \
  "ds_read_b128 v[64:67], v114 offset:17024\n\t" \
  "v_lshlrev_b32 v176, 16, v96\n\t" \
  "v_and_b32 v177, 0xffff0000, v96\n\t" \
  "v_lshlrev_b32 v178, 16, v97\n\t" \
  "v_and_b32 v179, 0xffff0000, v97\n\t" \
  "s_waitcnt lgkmcnt(3)\n\t" \
  "v_mfma_f32_32x32x16_bf16 v[224:239], v[52:55], v[148:151], v[224:239]\n\t" \
  "ds_read_b128 v[68:71], v114 offset:17056\n\t" \
  "v_lshlrev_b32 v180, 16, v98\n\t" \
  "v_and_b32 v181, 0xffff0000, v98\n\t" \
  "v_lshlrev_b32 v182, 16, v99\n\t" \
  "v_and_b32 v183, 0xffff0000, v99\n\t" \
  "s_waitcnt lgkmcnt(3)\n\t" \
  "v_mfma_f32_32x32x16_bf16 v[224:239], v[56:59], v[152:155], v[224:239]\n\t" \
  "ds_read_b128 v[72:75], v114 offset:17088\n\t" \
  "v_lshlrev_b32 v184, 16, v100\n\t" \
  "v_and_b32 v185, 0xffff0000, v100\n\t" \
  "v_lshlrev_b32 v186, 16, v101\n\t" \
  "v_and_b32 v187, 0xffff0000, v101\n\t" \
  "s_waitcnt lgkmcnt(3)\n\t" \
  "v_mfma_f32_32x32x16_bf16 v[224:239], v[60:63], v[156:159], v[224:239]\n\t" \
  "ds_read_b128 v[76:79], v114 offset:17120\n\t" \
  "v_lshlrev_b32 v188, 16, v102\n\t" \
  "v_and_b32 v189, 0xffff0000, v102\n\t" \
  "v_lshlrev_b32 v190, 16, v103\n\t" \
  "v_and_b32 v191, 0xffff0000, v103\n\t" \
  "s_waitcnt lgkmcnt(3)\n\t" \
  "v_mfma_f32_32x32x16_bf16 v[224:239], v[64:67], v[80:83], v[224:239]\n\t" \
  "ds_read_b128 v[48:51], v114 offset:17152\n\t" \
  "v_exp_f32 v240, v208\n\t" \
  "v_exp_f32 v241, v209\n\t" \
  "v_exp_f32 v242, v210\n\t" \
  "v_exp_f32 v243, v211\n\t" \
  "s_waitcnt lgkmcnt(3)\n\t" \
  "v_mfma_f32_32x32x16_bf16 v[224:239], v[68:71], v[84:87], v[224:239]\n\t" \
  "ds_read_b128 v[52:55], v114 offset:17184\n\t" \
  "v_exp_f32 v244, v212\n\t" \
  "v_exp_f32 v245, v213\n\t" \
  "v_exp_f32 v246, v214\n\t" \
  "v_exp_f32 v247, v215\n\t" \
  "s_waitcnt lgkmcnt(3)\n\t" \
  "v_mfma_f32_32x32x16_bf16 v[224:239], v[72:75], v[88:91], v[224:239]\n\t" \
  "ds_read_b128 v[56:59], v114 offset:17216\n\t" \
  "v_exp_f32 v248, v216\n\t" \
  "v_exp_f32 v249, v217\n\t" \
  "v_exp_f32 v250, v218\n\t" \
  "v_exp_f32 v251, v219\n\t" \
  "s_waitcnt lgkmcnt(3)\n\t" \
  "v_mfma_f32_32x32x16_bf16 v[224:239], v[76:79], v[92:95], v[224:239]\n\t" \
  "ds_read_b128 v[60:63], v114 offset:17248\n\t" \
  "v_exp_f32 v252, v220\n\t" \
  "v_exp_f32 v253, v221\n\t" \
  "v_exp_f32 v254, v222\n\t" \
  "v_exp_f32 v255, v223\n\t" \
  "s_waitcnt lgkmcnt(3)\n\t" \
  "v_mfma_f32_32x32x16_bf16 v[224:239], v[48:51], v[0:3], v[224:239]\n\t" \
  "ds_read_b128 v[64:67], v114 offset:17280\n\t" \
  "v_pk_fma_f32 v[240:241], v[240:241], s[78:79], s[78:79]\n\t" \
  "v_pk_fma_f32 v[242:243], v[242:243], s[78:79], s[78:79]\n\t" \
  "v_pk_fma_f32 v[244:245], v[244:245], s[78:79], s[78:79]\n\t" \
  "v_pk_fma_f32 v[246:247], v[246:247], s[78:79], s[78:79]\n\t" \
  "s_waitcnt lgkmcnt(3)\n\t" \
  "v_mfma_f32_32x32x16_bf16 v[224:239], v[52:55], v[4:7], v[224:239]\n\t" \
  "ds_read_b128 v[68:71], v114 offset:17312\n\t" \
  "v_pk_fma_f32 v[248:249], v[248:249], s[78:79], s[78:79]\n\t" \
  "v_pk_fma_f32 v[250:251], v[250:251], s[78:79], s[78:79]\n\t" \
  "v_pk_fma_f32 v[252:253], v[252:253], s[78:79], s[78:79]\n\t" \
  "v_pk_fma_f32 v[254:255], v[254:255], s[78:79], s[78:79]\n\t" \
  "s_waitcnt lgkmcnt(3)\n\t" \
  "v_mfma_f32_32x32x16_bf16 v[224:239], v[56:59], v[8:11], v[224:239]\n\t" \
  "ds_read_b128 v[72:75], v114 offset:17344\n\t" \
  "v_rcp_f32 v240, v240\n\t" \
  "v_rcp_f32 v241, v241\n\t" \
  "v_rcp_f32 v242, v242\n\t" \
  "v_rcp_f32 v243, v243\n\t" \
  "s_waitcnt lgkmcnt(3)\n\t" \
  "v_mfma_f32_32x32x16_bf16 v[224:239], v[60:63], v[12:15], v[224:239]\n\t" \
  "ds_read_b128 v[76:79], v114 offset:17376\n\t" \
  "v_rcp_f32 v244, v244\n\t" \
  "v_rcp_f32 v245, v245\n\t" \
  "v_rcp_f32 v246, v246\n\t" \
  "v_rcp_f32 v247, v247\n\t" \
  "s_waitcnt lgkmcnt(3)\n\t" \
  "v_mfma_f32_32x32x16_bf16 v[224:239], v[64:67], v[160:163], v[224:239]\n\t" \
  "v_rcp_f32 v248, v248\n\t" \
  "v_rcp_f32 v249, v249\n\t" \
  "v_rcp_f32 v250, v250\n\t" \
  "v_rcp_f32 v251, v251\n\t" \
  "s_waitcnt lgkmcnt(2)\n\t" \
  "v_mfma_f32_32x32x16_bf16 v[224:239], v[68:71], v[164:167], v[224:239]\n\t" \
  "v_rcp_f32 v252, v252\n\t" \
  "v_rcp_f32 v253, v253\n\t" \
  "v_rcp_f32 v254, v254\n\t" \
  "v_rcp_f32 v255, v255\n\t" \
  "s_waitcnt lgkmcnt(1)\n\t" \
  "v_mfma_f32_32x32x16_bf16 v[224:239], v[72:75], v[168:171], v[224:239]\n\t" \
  "v_pk_fma_f32 v[240:241], v[240:241], v[208:209], v[176:177]\n\t" \
  "v_pk_fma_f32 v[242:243], v[242:243], v[210:211], v[178:179]\n\t" \
  "v_pk_fma_f32 v[244:245], v[244:245], v[212:213], v[180:181]\n\t" \
  "v_pk_fma_f32 v[246:247], v[246:247], v[214:215], v[182:183]\n\t" \
  "s_waitcnt lgkmcnt(0)\n\t" \
  "v_mfma_f32_32x32x16_bf16 v[224:239], v[76:79], v[172:175], v[224:239]\n\t" \
  "v_pk_fma_f32 v[248:249], v[248:249], v[216:217], v[184:185]\n\t" \
  "v_pk_fma_f32 v[250:251], v[250:251], v[218:219], v[186:187]\n\t" \
  "v_pk_fma_f32 v[252:253], v[252:253], v[220:221], v[188:189]\n\t" \
  "v_pk_fma_f32 v[254:255], v[254:255], v[222:223], v[190:191]\n\t" \
  "v_cvt_pk_bf16_f32 v96, v240, v241\n\t" \
  "v_cvt_pk_bf16_f32 v97, v242, v243\n\t" \
  "v_cvt_pk_bf16_f32 v98, v244, v245\n\t" \
  "v_cvt_pk_bf16_f32 v99, v246, v247\n\t" \
  "v_cvt_pk_bf16_f32 v100, v248, v249\n\t" \
  "v_cvt_pk_bf16_f32 v101, v250, v251\n\t" \
  "v_cvt_pk_bf16_f32 v102, v252, v253\n\t" \
  "v_cvt_pk_bf16_f32 v103, v254, v255\n\t" \
  "global_store_dwordx4 v113, v[96:99], s[62:63] nt\n\t" \
  "global_store_dwordx4 v113, v[100:103], s[62:63] offset:32 nt\n\t" \
  "s_add_u32 s64, s62, 4194304\n\t" \
  "s_addc_u32 s65, s63, 0\n\t" \
  "v_exp_f32 v240, v224\n\t" \
  "v_exp_f32 v241, v225\n\t" \
  "v_exp_f32 v242, v226\n\t" \
  "v_exp_f32 v243, v227\n\t" \
  "v_add_u32 v108, 131072, v116\n\t" \
  "v_add_u32 v109, 262144, v116\n\t" \
  "v_pk_fma_f32 v[240:241], v[240:241], s[78:79], s[78:79]\n\t" \
  "v_pk_fma_f32 v[242:243], v[242:243], s[78:79], s[78:79]\n\t" \
  "v_add_u32 v127, 393216, v116\n\t" \
  "v_rcp_f32 v240, v240\n\t" \
  "v_rcp_f32 v241, v241\n\t" \
  "v_rcp_f32 v242, v242\n\t" \
  "v_rcp_f32 v243, v243\n\t" \
  "s_cmp_eq_u32 s43, 40\n\t" \
  "s_cselect_b32 s66, -1, 0\n\t" \
  "v_pk_fma_f32 v[240:241], v[240:241], v[224:225], v[104:105]\n\t" \
  "v_pk_fma_f32 v[242:243], v[242:243], v[226:227], v[106:107]\n\t" \
  "v_cvt_pk_bf16_f32 v240, v240, v241\n\t" \
  "v_cvt_pk_bf16_f32 v242, v242, v243\n\t" \
  "global_store_short v116, v240, s[64:65] nt\n\t" \
  "global_store_short_d16_hi v108, v240, s[64:65] nt\n\t" \
  "global_store_short v109, v242, s[64:65] nt\n\t" \
  "s_mov_b32 exec_hi, s66\n\t" \
  "global_store_short_d16_hi v127, v242, s[64:65] nt\n\t" \
  "s_mov_b32 exec_hi, -1\n\t" \
  "s_mov_b32 s51, 0\n\t" \
  "s_branch FIN_%=\n\t" \
  "SLOW_%=:\n\t" \
  "s_waitcnt vmcnt(0) lgkmcnt(0)\n\t" \
  "s_mov_b32 s51, 1\n\t" \
  "FIN_%=:\n\t" \
  ""

__global__ __launch_bounds__(512, 2) void egnn_edge(const unsigned short* __restrict__ h, const char* __restrict__ pos,
                                                    const char* __restrict__ eattr, const int* __restrict__ src,
                                                    const int* __restrict__ dst, const unsigned short* __restrict__ we,
                                                    const unsigned short* __restrict__ wx, const unsigned short* __restrict__ wh,
                                                    char* __restrict__ ws, unsigned short* __restrict__ out) {
  extern __shared__ __attribute__((aligned(16))) char smem[];
  const int wv = __builtin_amdgcn_readfirstlane(threadIdx.x >> 6);
  const int blk = blockIdx.x;
  const int nn = blk < NN - 39 * 256 ? 40 : 39;
  const char* Bp = ws + WS_B;
  STAMP_DECL; STAMP(0);
  int tid = threadIdx.x;
  unsigned vl = (tid & 63) * 16u;
  const int half = (blk >> 3) & 1, ti = 15 - (blk >> 4), rt = 14 * (blk & 7) + ti;
  const bool isg = ti < 14 && rt < GT;
  const int ch = ti >= 14 ? blk : 32 + (rt - GT) * 2 + half;
  const bool isb = !isg && ch < NCH, leader = !isg && ch == NCH + 1;
  unsigned* arr = (unsigned*)(ws + WS_ARR);
  unsigned* gow = (unsigned*)(ws + WS_REL);
  u32x2 gt;
  unsigned go0, ticket = 0;
  if (leader && wv == 0) {
    u32x4 a;
    u32x2 r0;
    GLDC("global_load_dwordx4", a, vl, arr);
    GLDC("global_load_dwordx2", r0, (vl & (NREL * 16u - 1u)) * 8u, gow);
    asm volatile("s_waitcnt vmcnt(0)" : "+v"(a), "+v"(r0) :: "memory");
    ticket = __builtin_amdgcn_readfirstlane(a.x);
    do ticket++; while (__any(a.x == ticket || a.y == ticket || a.z == ticket || a.w == ticket || r0.x == ticket || r0.y == ticket));
    if (vl < NREL * 16u) __hip_atomic_store(gow + vl * 2u + 1, ticket, __ATOMIC_RELAXED, __HIP_MEMORY_SCOPE_AGENT);
    if (vl == 0) *LDSP(unsigned, L_END + 72) = ticket;
  }
  if (wv == 3) *LDSP(u32x4, L_POS + vl) = u32x4{0u, 0u, 0u, 0u};
  u32x4 wah, wal, aw1;
  u32x8 aw0;
  u32x2 aw2;
  u32x16 wxf;
  unsigned role = __builtin_amdgcn_readfirstlane(isg ? 0 : (isb ? 1 : 2));
  ticket = __builtin_amdgcn_readfirstlane(ticket);
  asm volatile(PHASE1_ASM
               : "={v[88:91]}"(wah), "={v[92:95]}"(wal), "={v[96:103]}"(aw0), "={v[104:107]}"(aw1), "={v[108:109]}"(aw2), "={v[128:143]}"(wxf),
                 "+{v111}"(vl), "={v[126:127]}"(gt), "+{s54}"(role), "+{s58}"(ticket), "={s60}"(go0)
               : "{s[36:37]}"(h), "{s[38:39]}"(we), "{s[40:41]}"(wh), "{s[42:43]}"(ws), "{s[44:45]}"(src), "{s[46:47]}"(dst), "{s[48:49]}"(pos), "{s[50:51]}"(wx), "{s[72:73]}"(eattr),
                 "{s52}"(blk), "{s53}"(wv), "{s55}"(rt), "{s56}"(half), "{s57}"(ch), "{s59}"(__builtin_amdgcn_readfirstlane(leader ? 1 : 0))
               : "v0", "v1", "v2", "v3", "v4", "v5", "v6", "v7", "v8", "v9", "v10", "v11", "v12", "v13", "v14", "v15", "v16", "v17",
                   "v18", "v19", "v20", "v21", "v22", "v23", "v24", "v25", "v26", "v27", "v28", "v29", "v30", "v31",
                   "v32", "v33", "v34", "v35", "v36", "v37", "v38", "v39", "v40", "v41", "v42", "v43", "v44", "v45",
                   "v46", "v47", "v48", "v49", "v50", "v51", "v52", "v53", "v54", "v55", "v56", "v57", "v58", "v59",
                   "v60", "v61", "v62", "v63", "v64", "v65", "v66", "v67", "v68", "v69", "v70", "v71", "v72", "v73",
                   "v74", "v75", "v76", "v77", "v78", "v79", "v80", "v81", "v82", "v83", "v84", "v85", "v86", "v87",
                   "v110", "v112", "v113", "v114", "v115", "v116", "v117", "v118", "v119", "v120", "v121", "v122",
                   "v123", "v124", "v125", "v144", "v145", "v146", "v147", "v148", "v149", "v150",
                   "v151", "v152", "v153", "v154", "v155", "v156", "v157", "v158", "v159", "v160", "v161", "v162",
                   "v163", "v164", "v165", "v166", "v167", "v168", "v169", "v170", "v171", "v172", "v173", "v174",
                   "v175", "v176", "v177", "v178", "v179", "v180", "v181", "v182", "v183", "v184", "v185", "v186",
                   "v187", "v188", "v189", "v190", "v191", "v192", "v193", "v194", "v195", "v196", "v197", "v198",
                   "v199", "v200", "v201", "v202", "v203", "v204", "v205", "v206", "v207", "v208", "v209", "v210",
                   "v211", "v212", "v213", "v214", "v215", "v216", "v217", "v218", "v219", "v220", "v221", "v222",
                   "v223", "v224", "v225", "v226", "v227", "v228", "v229", "v230", "v231", "v232", "v233", "v234",
                   "v235", "v236", "v237", "v238", "v239", "v240", "v241", "v242", "v243", "v244", "v245", "v246",
                   "v247", "v248", "v249", "v250", "v251", "v252", "v253", "v254", "v255",
                   "s62", "s63", "s64", "s65", "s66", "s67", "s68", "s69", "s70", "s71", "vcc", "scc", "memory");
  typedef const void* const __attribute__((address_space(4)))* kargs_t;
  kargs_t ka = (kargs_t)__builtin_amdgcn_kernarg_segment_ptr();
  asm volatile("" : "+s"(ka));
  h = (const unsigned short*)ka[0]; pos = (const char*)ka[1]; eattr = (const char*)ka[2]; ws = (char*)ka[8]; out = (unsigned short*)ka[9];
  arr = (unsigned*)(ws + WS_ARR); gow = (unsigned*)(ws + WS_REL); Bp = ws + WS_B;
  STAMP(3);
  if (leader) {
    if (wv < 4) {
      ticket = __builtin_amdgcn_readfirstlane(*LDSP(unsigned, L_END + 72));
      for (int spin = 0; spin < (1 << 22); spin++) {
        u32x4 a;
        GLDC("global_load_dwordx4", a, vl, arr);
        asm volatile("s_waitcnt vmcnt(0)" : "+v"(a) :: "memory");
        if (__all(a.x == ticket && a.y == ticket && a.z == ticket && a.w == ticket)) break;
      }
      if (vl < NREL * 16u) __hip_atomic_store(gow + vl * 2u, ticket, __ATOMIC_RELAXED, __HIP_MEMORY_SCOPE_AGENT);
    }
  } else if (wv == 0) {
    for (int spin = 0; spin < (1 << 22); spin++) {
      if ((unsigned)__builtin_amdgcn_readfirstlane(gt.x) != go0) break;
      const unsigned t = __builtin_amdgcn_readfirstlane(gt.y);
      if (t != ticket) {
        ticket = t;
        if (vl == 0) __hip_atomic_store(arr + blk, ticket, __ATOMIC_RELAXED, __HIP_MEMORY_SCOPE_AGENT);
      }
      GLDC("global_load_dwordx2", gt, 0u, gow + (blk & (NREL - 1)) * 32);
      asm volatile("s_waitcnt vmcnt(0)" : "+v"(gt) :: "memory");
    }
  }
  BAR();
  STAMP(4);
  u32x16 sa, sb;
  {
    const unsigned woff = 2 * wv * PAIR_PITCH;
#pragma unroll
    for (int j = 0; j < 16; j++) { sa[j] = 0u; sb[j] = 0u; }
    sa[3] = woff; sa[4] = wv < 4 ? 0x01010100u : 0x10101010u; sa[7] = nn;
    sa[13] = 4 * SLOT_SZ + 2 * woff; sa[14] = 2 * SLOT_SZ + 2 * woff;
    const char* whp = ws + WS_WH + wv * 32768;
    const char* outp = (const char*)out + blk * 512 + wv * 64;
    sb[0] = (unsigned)(uintptr_t)Bp; sb[1] = (unsigned)((uintptr_t)Bp >> 32);
    sb[2] = (unsigned)(uintptr_t)eattr; sb[3] = (unsigned)((uintptr_t)eattr >> 32);
    sb[4] = (unsigned)(uintptr_t)h; sb[5] = (unsigned)((uintptr_t)h >> 32);
    sb[6] = (unsigned)(uintptr_t)whp; sb[7] = (unsigned)((uintptr_t)whp >> 32);
    sb[8] = blk; sb[9] = wv;
    sb[10] = (unsigned)(uintptr_t)outp; sb[11] = (unsigned)((uintptr_t)outp >> 32);
    STAMP_PIN;
    asm volatile(EDGE_LOOP_ASM
                 : "+{s[36:51]}"(sa), "+{s[52:67]}"(sb), "+{v[88:91]}"(wah), "+{v[92:95]}"(wal),
                   "+{v[96:103]}"(aw0), "+{v[104:107]}"(aw1), "+{v[108:109]}"(aw2), "+{v111}"(vl), "+{v[128:143]}"(wxf)
                 : "{s[80:81]}"(ws + WS_TBL + blk * 2048), "{s[82:83]}"(ws + WS_REC + blk * (NCH * 256)), "{s[84:85]}"(pos)
                 : "v0", "v1", "v2", "v3", "v4", "v5", "v6", "v7", "v8", "v9", "v10", "v11", "v12", "v13", "v14", "v15", "v16", "v17",
                   "v18", "v19", "v20", "v21", "v22", "v23", "v24", "v25", "v26", "v27", "v28", "v29", "v30", "v31",
                   "v32", "v33", "v34", "v35", "v36", "v37", "v38", "v39", "v40", "v41", "v42", "v43", "v44", "v45",
                   "v46", "v47", "v48", "v49", "v50", "v51", "v52", "v53", "v54", "v55", "v56", "v57", "v58", "v59",
                   "v60", "v61", "v62", "v63", "v64", "v65", "v66", "v67", "v68", "v69", "v70", "v71", "v72", "v73",
                   "v74", "v75", "v76", "v77", "v78", "v79", "v80", "v81", "v82", "v83", "v84", "v85", "v86", "v87",
                   "v110", "v112", "v113", "v114", "v115", "v116", "v117", "v118", "v119", "v120", "v121", "v122",
                   "v123", "v124", "v125", "v126", "v127", "v144", "v145", "v146", "v147", "v148", "v149", "v150",
                   "v151", "v152", "v153", "v154", "v155", "v156", "v157", "v158", "v159", "v160", "v161", "v162",
                   "v163", "v164", "v165", "v166", "v167", "v168", "v169", "v170", "v171", "v172", "v173", "v174",
                   "v175", "v176", "v177", "v178", "v179", "v180", "v181", "v182", "v183", "v184", "v185", "v186",
                   "v187", "v188", "v189", "v190", "v191", "v192", "v193", "v194", "v195", "v196", "v197", "v198",
                   "v199", "v200", "v201", "v202", "v203", "v204", "v205", "v206", "v207", "v208", "v209", "v210",
                   "v211", "v212", "v213", "v214", "v215", "v216", "v217", "v218", "v219", "v220", "v221", "v222",
                   "v223", "v224", "v225", "v226", "v227", "v228", "v229", "v230", "v231", "v232", "v233", "v234",
                   "v235", "v236", "v237", "v238", "v239", "v240", "v241", "v242", "v243", "v244", "v245", "v246",
                   "v247", "v248", "v249", "v250", "v251", "v252", "v253", "v254", "v255",
                   "s68", "s69", "s70", "s71", "s72", "s73", "s74", "s75", "s76", "s77", "s78", "s79", "s86", "s87", "vcc", "scc", "memory");
  }
  tid = wv * 64 + (int)(vl >> 4);
  asm volatile("" : "+s"(ka));
  out = (unsigned short*)ka[9];
  if (__builtin_expect(sa[15] == 0u, 1)) {
  } else {
    h = (const unsigned short*)ka[0]; pos = (const char*)ka[1]; eattr = (const char*)ka[2]; src = (const int*)ka[3]; dst = (const int*)ka[4];
    we = (const unsigned short*)ka[5]; wx = (const unsigned short*)ka[6]; wh = (const unsigned short*)ka[7];
    Bp = (const char*)ka[8] + WS_B;
    float* accm = (float*)smem;
    float* arow = (float*)(smem + 40960);
    float* red = (float*)(smem + 81920);
    float* pacc = (float*)(smem + 81920 + 64);
    int* lst = (int*)(smem + 83968);
    __syncthreads();
    const int f = tid & 255;
    for (int r = tid >> 8; r < 40; r += 2) {
      float a = 0.f;
      for (int k = 0; k < 256; k++) a += bf2f(h[(size_t)min(blk + 256 * r, NN - 1) * 256 + k]) * bf2f(we[(size_t)k * 256 + f]);
      arow[r * 256 + f] = a * NLOG2E;
      accm[r * 256 + f] = 0.f;
    }
    if (tid < 192) pacc[tid] = 0.f;
    const float wdv = bf2f(we[(size_t)512 * 256 + f]) * NLOG2E, wxs = bf2f(wx[f]) * NLN2;
    for (int base = 0; base < NE; base += 512) {
      if (tid == 0) lst[512] = 0;
      __syncthreads();
      const int e = base + tid;
      if (e < NE && (dst[e] & 255) == blk) lst[atomicAdd(&lst[512], 1)] = e;
      __syncthreads();
      const int cnt = lst[512];
      for (int i = 0; i < cnt; i++) {
        const int ed = lst[i];
        const unsigned dn = dst[ed], sn = src[ed], loc = dn >> 8;
        unsigned dlo, dhi, slo, shi, xy, zd;
        unshift(*(const u32x2*)(pos + ((dn * 6u) & ~3u)), dn, dlo, dhi);
        unshift(*(const u32x2*)(pos + ((sn * 6u) & ~3u)), sn, slo, shi);
        geom(dlo, dhi, slo, shi, xy, zd);
        if (tid < 256) {
          float y = bf2f(zd >> 16) * wdv;
          const unsigned short* ea = (const unsigned short*)(eattr + ed * 32u);
          for (int k = 0; k < 16; k++) y += bf2f(ea[k]) * (bf2f(we[(size_t)(513 + k) * 256 + f]) * NLOG2E);
          y += arow[loc * 256 + f] + bf2f(__hip_atomic_load((const unsigned short*)(Bp + sn * 512u + 2 * f), __ATOMIC_RELAXED, __HIP_MEMORY_SCOPE_AGENT));
          const float m = rbf(y * __builtin_amdgcn_rcpf(1.0f + __builtin_amdgcn_exp2f(y)));
          accm[loc * 256 + f] += m;
          float cs = m * wxs;
          for (int o = 32; o > 0; o >>= 1) cs += __shfl_xor(cs, o);
          if ((tid & 63) == 0) red[wv] = cs;
        }
        __syncthreads();
        if (tid < 3) {
          const float cs = red[0] + red[1] + red[2] + red[3];
          const float dv = tid == 0 ? bf2f(xy & 0xffffu) : tid == 1 ? bf2f(xy >> 16) : bf2f(zd & 0xffffu);
          pacc[(loc + ((loc * 37u) >> 8)) * 4 + 1 + tid] += dv * cs;
        }
        __syncthreads();
      }
      __syncthreads();
    }
    for (int i = tid; i < 6 * 512; i += 512) *LDSP(float, L_PP + i * 4u) = tid < 32 ? pacc[(i >> 9) * 32 + tid] : 0.f;
    for (int r = tid >> 8; r < nn; r += 2) {
      float zh = 0.f, zm = 0.f;
      for (int k = 0; k < 256; k++) {
        zh += bf2f(h[(size_t)(blk + 256 * r) * 256 + k]) * bf2f(wh[(size_t)k * 256 + f]);
        zm += rbf(accm[r * 256 + k]) * bf2f(wh[(size_t)(256 + k) * 256 + f]);
      }
      const float y = zh * NLOG2E + zm;
      out[(size_t)(blk + 256 * r) * 256 + f] = (unsigned short)pk2(bf2f(h[(size_t)(blk + 256 * r) * 256 + f]) + NLN2 * y * __builtin_amdgcn_rcpf(1.0f + __builtin_amdgcn_exp2f(y)), 0.f);
    }
    __syncthreads();
  }
  if (tid < 3 * nn) {
    const int r = tid / 3, d = tid - 3 * r, w7 = (r * 37) >> 8;
    float s = 0.f;
#pragma unroll
    for (int w = 0; w < 16; w++) s += *LDSP(float, L_PP + w7 * 2048u + w * 128u + (r - 7 * w7) * 16u + (d + 1) * 4u);
    out[(size_t)NN * 256 + (blk + 256 * r) * 3 + d] = (unsigned short)pk2(*LDSP(float, L_POS + (r + w7) * 16u + d * 4u) + rbf(s), 0.f);
  }
  STAMP(10); STAMP_OUT(0);
}

extern "C" void kernel_launch(void* const* d_in, const int* in_sizes, int n_in, void* d_out, int out_size, void* d_ws,
                              size_t ws_size, hipStream_t stream) {
  const unsigned short* h = (const unsigned short*)d_in[0];
  const char* pos = (const char*)d_in[1];
  const char* eattr = (const char*)d_in[2];
  const int* src = (const int*)d_in[3];
  const int* dst = (const int*)d_in[4];
  const unsigned short* we = (const unsigned short*)d_in[5];
  const unsigned short* wx = (const unsigned short*)d_in[6];
  const unsigned short* wh = (const unsigned short*)d_in[7];
  (void)hipFuncSetAttribute((const void*)egnn_edge, hipFuncAttributeMaxDynamicSharedMemorySize, 163840);
  egnn_edge<<<NBLK, 512, 163840, stream>>>(h, pos, eattr, src, dst, we, wx, wh, (char*)d_ws, (unsigned short*)d_out);
}
```
